```python
import torch

def _softmax(x, axis=-1):
    return torch.softmax(x, dim=axis)
def _gelu(x):
    return torch.nn.functional.gelu(x, approximate="tanh")
def _silu(x):
    return torch.nn.functional.silu(x)
def _sigmoid(x):
    return torch.sigmoid(x)
def _layernorm(x, axis=-1, eps=1e-5):
    m = x.mean(dim=axis, keepdim=True)
    v = x.var(dim=axis, keepdim=True, unbiased=False)
    return (x - m) / torch.sqrt(v + eps)
def _scatter_add(tgt, idx, src, axis=0):
    return tgt.clone().index_add_(axis, idx, src)
def _segment_sum(src, seg, n):
    out = torch.zeros((n,) + src.shape[1:], dtype=src.dtype)
    return out.index_add_(0, seg, src)
def _pad(x, pw):
    flat = tuple(v for pair in reversed(pw) for v in pair)
    return torch.nn.functional.pad(x, flat)

kernel_name = 'parallel_sum__gqa_block_cached__dense_activation_gelu__decode'

def setup_inputs():

    _w = (lambda a: (lambda t: t.to(torch.bfloat16) if t.is_floating_point() else t)(torch.as_tensor(a)))
    def _A_setup():
        B, Sq, D = 2, 1, 512
        Hq, Hkv, Sk, Dh = 32, 8, 8192, 128
        assert Hq % Hkv == 0, (Hq, Hkv)
        return {
            "x": _w((lambda *s: torch.randn(*s, dtype=torch.bfloat16) * 0.05)(B, Sq, D)),
            "k_cache": _w((lambda *s: torch.randn(*s, dtype=torch.bfloat16) * 1.6)(B, Hkv, Sk, Dh)),
            "v_cache": _w((lambda *s: torch.randn(*s, dtype=torch.bfloat16) * 0.8)(B, Hkv, Sk, Dh)),
            "wq": _w((lambda *s: torch.randn(*s, dtype=torch.bfloat16) * 1.6)(D, Hq * Dh) * (D ** -0.5)),
            "wo": _w((lambda *s: torch.randn(*s, dtype=torch.bfloat16) * 0.8)(Hq * Dh, D) * ((Hq * Dh) ** -0.5)),
        }
    _A_in = _A_setup()

    def _B_setup():
        B, S, D, F = 2, 1, 512, 1792
        return {
            "x": _w((lambda *s: torch.randn(*s, dtype=torch.bfloat16) * 0.05)(B, S, D)),
            "w1": _w((lambda *s: torch.randn(*s, dtype=torch.bfloat16) * 0.4)(D, F) * (D ** -0.5)),
            "b1": _w((lambda *s: torch.randn(*s, dtype=torch.bfloat16) * 0.4)(F) * 0.01),
            "w2": _w((lambda *s: torch.randn(*s, dtype=torch.bfloat16) * 0.4)(F, D) * (F ** -0.5)),
            "b2": _w((lambda *s: torch.randn(*s, dtype=torch.bfloat16) * 0.4)(D) * 0.01),
        }
    _B_in = _B_setup()

    return {
        "A_x": _A_in["x"],
        "A_k_cache": _A_in["k_cache"],
        "A_v_cache": _A_in["v_cache"],
        "A_wq": _A_in["wq"],
        "A_wo": _A_in["wo"],
        "B_x": _B_in["x"],
        "B_w1": _B_in["w1"],
        "B_b1": _B_in["b1"],
        "B_w2": _B_in["w2"],
        "B_b2": _B_in["b2"],
    }

def reference(A_x, A_k_cache, A_v_cache, A_wq, A_wo, B_x, B_w1, B_b1, B_w2, B_b2):
    def _A_ref(x, k_cache, v_cache, wq, wo):
        B, Sq, D = x.shape
        Hq, Dh = 32, 128
        Hkv = k_cache.shape[1]
        rep = Hq // Hkv
        q = (lambda x, *d: x.permute(*d))((x @ wq).reshape(B, Sq, Hq, Dh), 0, 2, 1, 3)
        k = (lambda x, n, axis: torch.repeat_interleave(x, n, dim=axis))(k_cache, rep, axis=1)
        v = (lambda x, n, axis: torch.repeat_interleave(x, n, dim=axis))(v_cache, rep, axis=1)
        scores = torch.einsum("bhqd,bhkd->bhqk", q, k) * (Dh ** -0.5)
        p = _softmax(scores, axis=-1)
        o = torch.einsum("bhqk,bhkd->bhqd", p, v)
        return (lambda x, *d: x.permute(*d))(o, 0, 2, 1, 3).reshape(B, Sq, Hq * Dh) @ wo

    def _B_ref(x, w1, b1, w2, b2):
        h = torch.einsum("bsd,df->bsf", x, w1) + b1
        h = _gelu(h)
        return torch.einsum("bsf,fd->bsd", h, w2) + b2

    x = A_x
    xn = _layernorm(x, axis=-1)
    return x + _A_ref(xn, A_k_cache, A_v_cache, A_wq, A_wo) + _B_ref(xn, B_w1, B_b1, B_w2, B_b2)
```

```cpp
#include <hip/hip_runtime.h>
#include <stdint.h>

typedef unsigned short u16;
typedef unsigned long long u64;
typedef unsigned v4u __attribute__((ext_vector_type(4)));
typedef unsigned v2u __attribute__((ext_vector_type(2)));
typedef float f32x4 __attribute__((ext_vector_type(4)));
typedef float f32x2 __attribute__((ext_vector_type(2)));
typedef __bf16 bf16x8 __attribute__((ext_vector_type(8)));
typedef __bf16 bf16x2 __attribute__((ext_vector_type(2)));
#define GAS __attribute__((address_space(1)))
#define RLX_AGENT __ATOMIC_RELAXED, __HIP_MEMORY_SCOPE_AGENT

#define KWIN 4
#define KV_NT 1

constexpr int WS_EPOCH = 0;
constexpr int WS_FLAGS = 4096;
constexpr int WS_QG = 8192;
constexpr int WS_BLK = 131072;
constexpr int BLK_SZ = 4608, BLK_IN1 = 0, BLK_IN2 = 2048, BLK_ML = 4096;
constexpr int WS_BYTES = WS_BLK + 256 * BLK_SZ;
constexpr int WS_STAMP = 16 << 20;
constexpr unsigned SENT = 0xFFFFFFFFu;
constexpr int AUX_SC1 = 16;
constexpr unsigned SPIN_CAP = 1u << 18;

template <int CTRL> __device__ __forceinline__ float dpp(float x) {
    return __builtin_bit_cast(float, __builtin_amdgcn_mov_dpp(__builtin_bit_cast(int, x), CTRL, 0xf, 0xf, true));
}
constexpr int XOR1 = 0xB1, XOR2 = 0x4E, HMIR = 0x141, ROR8 = 0x128, ROR4 = 0x124, ROR2 = 0x122;
__device__ __forceinline__ float sw32_add(float a, float b) {
    auto r = __builtin_amdgcn_permlane32_swap(__float_as_uint(a), __float_as_uint(b), false, false);
    return __uint_as_float(r[0]) + __uint_as_float(r[1]);
}
__device__ __forceinline__ float sw16_add(float a, float b) {
    auto r = __builtin_amdgcn_permlane16_swap(__float_as_uint(a), __float_as_uint(b), false, false);
    return __uint_as_float(r[0]) + __uint_as_float(r[1]);
}
__device__ __forceinline__ float sw32_max(float a) {
    auto r = __builtin_amdgcn_permlane32_swap(__float_as_uint(a), __float_as_uint(a), false, false);
    return fmaxf(__uint_as_float(r[0]), __uint_as_float(r[1]));
}
__device__ __forceinline__ float sw16_max(float a) {
    auto r = __builtin_amdgcn_permlane16_swap(__float_as_uint(a), __float_as_uint(a), false, false);
    return fmaxf(__uint_as_float(r[0]), __uint_as_float(r[1]));
}
__device__ __forceinline__ float row_sum(float x) { x += dpp<XOR1>(x); x += dpp<XOR2>(x); x += dpp<HMIR>(x); x += dpp<ROR8>(x); return x; }
__device__ __forceinline__ float oct_sum(float x) { x += dpp<XOR1>(x); x += dpp<XOR2>(x); x += dpp<HMIR>(x); return x; }
__device__ __forceinline__ float oct_max(float x) { x = fmaxf(x, dpp<XOR1>(x)); x = fmaxf(x, dpp<XOR2>(x)); x = fmaxf(x, dpp<HMIR>(x)); return x; }
__device__ __forceinline__ float wave_sum(float x) { x = row_sum(x); x = sw16_add(x, x); x = sw32_add(x, x); return x; }
__device__ __forceinline__ float bflo(unsigned u) { return __uint_as_float(u << 16); }
__device__ __forceinline__ float bfhi(unsigned u) { return __uint_as_float(u & 0xffff0000u); }
__device__ __forceinline__ unsigned pk_bf16(float lo, float hi) {
    f32x2 v = {lo, hi};
    return __builtin_bit_cast(unsigned, __builtin_convertvector(v, bf16x2));
}
__device__ __forceinline__ float rbf(float x) { return bflo(pk_bf16(x, x)); }
__device__ __forceinline__ f32x4 mfma(v4u a, v4u b, f32x4 c) {
    return __builtin_amdgcn_mfma_f32_16x16x32_bf16(__builtin_bit_cast(bf16x8, a), __builtin_bit_cast(bf16x8, b), c, 0, 0, 0);
}
__device__ __forceinline__ v4u ldkv(const unsigned char* p) {
    return __builtin_nontemporal_load((const v4u*)p);
}
__device__ __forceinline__ v4u nosent(f32x4 v) {
    v4u u = __builtin_bit_cast(v4u, v);
    u.x = min(u.x, SENT - 1u); u.y = min(u.y, SENT - 1u); u.z = min(u.z, SENT - 1u); u.w = min(u.w, SENT - 1u);
    return u;
}
__device__ __forceinline__ unsigned max4(v4u a) { return max(max(a.x, a.y), max(a.z, a.w)); }
#define SB() __builtin_amdgcn_sched_barrier(0)
#define CFENCE() asm volatile("" ::: "memory")
#define BAR() do { asm volatile("s_waitcnt lgkmcnt(0)" ::: "memory"); __builtin_amdgcn_s_barrier(); asm volatile("" ::: "memory"); } while (0)
#define STR2(x) #x
#define STR(x) STR2(x)

#define STAMP(i) do {} while (0)

__global__ void __launch_bounds__(512, 2)
fused_decode(const u16* __restrict__ x, const u16* __restrict__ Kc, const u16* __restrict__ Vc, const u16* __restrict__ wq,
             const u16* __restrict__ wo, const u16* __restrict__ w1, const u16* __restrict__ b1, const u16* __restrict__ w2,
             const u16* __restrict__ b2, u16* __restrict__ out, unsigned char* __restrict__ ws) {
    __shared__ __attribute__((aligned(16))) float s_ored[8 * 128 * 4];
    __shared__ __attribute__((aligned(16))) float s_qred[8 * 32];
    __shared__ __attribute__((aligned(16))) float s_hred[8 * 16];
    __shared__ __attribute__((aligned(16))) unsigned s_q[256];
    __shared__ __attribute__((aligned(16))) unsigned s_h[16];
    __shared__ __attribute__((aligned(16))) float s_ml[8 * 4 * 2];
    __shared__ unsigned s_flag;

    const int bid = blockIdx.x, kvh = bid & 7, rr = bid >> 3, b = rr >> 4, s = rr & 15;
    const int tid = threadIdx.x, lane = tid & 63, w = __builtin_amdgcn_readfirstlane(tid >> 6);
    const int L = b * 128 + kvh * 16 + s, grp = b * 8 + kvh;
    const __amdgpu_buffer_rsrc_t rs = __builtin_amdgcn_make_buffer_rsrc(ws, 0, WS_BYTES, 0x00020000);
    STAMP(0);
    if (tid < 264) __builtin_amdgcn_raw_buffer_store_b128(v4u{SENT, SENT, SENT, SENT}, rs, WS_BLK + L * BLK_SZ + tid * 16, 0, AUX_SC1);
    if (tid == 0) s_flag = 0;
    CFENCE();
    const u64 epoch = __hip_atomic_load((GAS u64*)(ws + WS_EPOCH + b * 256), RLX_AGENT);
    const u16* xb = x + b * 512;
    const v4u xa = *(const v4u*)(xb + 8 * lane);
    const v2u xq2 = *(const v2u*)(xb + 64 * w + 4 * (lane >> 2));
    const unsigned xm2 = *(const unsigned*)(xb + 64 * w + 2 * (lane >> 1));
    v4u wqr[4];
    {
        const u16* p = wq + (size_t)(64 * w) * 4096 + kvh * 512 + s * 32;
        const unsigned lo = (unsigned)(4 * (lane >> 2)) * 4096u + 8u * (lane & 3);
#pragma unroll
        for (int i = 0; i < 4; ++i) wqr[i] = *(const v4u*)(p + lo + i * 4096);
    }
    const int sm = s < 14 ? s : s - 2;
    const int hid0 = kvh * 224 + sm * 16;
    v4u w1r[2];
    {
        const u16* p = w1 + (size_t)(64 * w) * 1792 + hid0;
        const unsigned lo = (unsigned)(2 * (lane >> 1)) * 1792u + 8u * (lane & 1);
#pragma unroll
        for (int i = 0; i < 2; ++i) w1r[i] = *(const v4u*)(p + lo + i * 1792);
    }
    const unsigned b1v = b1[hid0 + (lane & 15)];
    const int c2 = kvh * 16 + s;
    const int rho = lane >> 4;
    const int ocol = 64 * (c2 >> 4) + 32 * ((c2 >> 3) & 1) + 8 * (2 * (rho & 1) + (rho >> 1)) + (c2 & 7);
    const unsigned xo = xb[ocol], b2o = b2[ocol];
    SB();
    const size_t kvoff = ((size_t)(b * 8 + kvh) * 8192 + s * 512 + w * 64) * 256;
    const unsigned char* kb = (const unsigned char*)Kc + kvoff;
    const unsigned char* vb = (const unsigned char*)Vc + kvoff;
    const unsigned r16 = lane & 15;
    const unsigned klo = (4u * (r16 & 3) + (r16 >> 2)) * 256u + 16u * (lane >> 4);
    const unsigned vlo = 16u * lane;
    v4u kr[16], vr[16];
#pragma unroll
    for (int i = 0; i < KWIN; ++i) kr[i] = ldkv(kb + klo + (i >> 2) * 4096 + (i & 3) * 64);
    SB();
    float xs[8];
#pragma unroll
    for (int k = 0; k < 4; ++k) { xs[2 * k] = bflo(xa[k]); xs[2 * k + 1] = bfhi(xa[k]); }
    float sum = ((xs[0] + xs[1]) + (xs[2] + xs[3])) + ((xs[4] + xs[5]) + (xs[6] + xs[7]));
    const float mean = wave_sum(sum) * (1.0f / 512.0f);
    float sq = 0.f;
#pragma unroll
    for (int k = 0; k < 8; ++k) { const float d = xs[k] - mean; sq = fmaf(d, d, sq); }
    const float var = wave_sum(sq) * (1.0f / 512.0f);
    const float rstd = 1.0f / sqrtf(var + 1e-5f);
    {
        float xn[4] = {rbf((bflo(xq2.x) - mean) * rstd), rbf((bfhi(xq2.x) - mean) * rstd), rbf((bflo(xq2.y) - mean) * rstd), rbf((bfhi(xq2.y) - mean) * rstd)};
        float a[8];
#pragma unroll
        for (int k = 0; k < 8; ++k) a[k] = 0.f;
#pragma unroll
        for (int i = 0; i < 4; ++i)
#pragma unroll
            for (int k = 0; k < 4; ++k) { a[2 * k] = fmaf(xn[i], bflo(wqr[i][k]), a[2 * k]); a[2 * k + 1] = fmaf(xn[i], bfhi(wqr[i][k]), a[2 * k + 1]); }
        float t[4], u[2];
#pragma unroll
        for (int j = 0; j < 4; ++j) t[j] = sw32_add(a[j], a[j + 4]);
#pragma unroll
        for (int j = 0; j < 2; ++j) { u[j] = sw16_add(t[j], t[j + 2]); u[j] += dpp<ROR8>(u[j]); u[j] += dpp<ROR4>(u[j]); }
        if (r16 < 4) *(f32x2*)&s_qred[w * 32 + 8 * r16 + 2 * (rho & 1) + 4 * (rho >> 1)] = f32x2{u[0], u[1]};
    }
    {
        float xn[2] = {rbf((bflo(xm2) - mean) * rstd), rbf((bfhi(xm2) - mean) * rstd)};
        float a[8];
#pragma unroll
        for (int k = 0; k < 8; ++k) a[k] = 0.f;
#pragma unroll
        for (int i = 0; i < 2; ++i)
#pragma unroll
            for (int k = 0; k < 4; ++k) { a[2 * k] = fmaf(xn[i], bflo(w1r[i][k]), a[2 * k]); a[2 * k + 1] = fmaf(xn[i], bfhi(w1r[i][k]), a[2 * k + 1]); }
        float t[4], u[2];
#pragma unroll
        for (int j = 0; j < 4; ++j) t[j] = sw32_add(a[j], a[j + 4]);
#pragma unroll
        for (int j = 0; j < 2; ++j) { u[j] = sw16_add(t[j], t[j + 2]); u[j] += dpp<ROR8>(u[j]); u[j] += dpp<ROR4>(u[j]); u[j] += dpp<ROR2>(u[j]); }
        if (r16 < 2) *(f32x2*)&s_hred[w * 16 + 8 * r16 + 2 * (rho & 1) + 4 * (rho >> 1)] = f32x2{u[0], u[1]};
    }
    STAMP(1);
    BAR();
    const u64 tag = epoch + 1ull;
    if (w == 0) {
        float qv = 0.f;
#pragma unroll
        for (int k = 0; k < 8; ++k) qv += s_qred[k * 32 + (lane & 31)];
        const u64 gran = (tag << 16) | (u64)(pk_bf16(qv, qv) & 0xffffu);
        if (lane < 32) __hip_atomic_store((GAS u64*)(ws + WS_QG) + grp * 512 + s * 32 + lane, gran, RLX_AGENT);
        else if (lane == 32) __hip_atomic_store((GAS u64*)(ws + WS_FLAGS) + L, tag, RLX_AGENT);
        STAMP(2);
        const unsigned thi = (unsigned)(tag >> 16), tlo = (unsigned)(tag & 0xffffu);
        v4u g[4];
        for (unsigned spins = 0;; ++spins) {
            CFENCE();
#pragma unroll
            for (int k = 0; k < 4; ++k) g[k] = __builtin_amdgcn_raw_buffer_load_b128(rs, WS_QG + grp * 4096 + k * 1024 + lane * 16, 0, AUX_SC1);
            bool ok = true;
#pragma unroll
            for (int k = 0; k < 4; ++k) ok = ok && g[k].y == thi && g[k].w == thi && (g[k].x >> 16) == tlo && (g[k].z >> 16) == tlo;
            if (__all(ok) || spins > SPIN_CAP) break;
        }
#pragma unroll
        for (int k = 0; k < 4; ++k) s_q[k * 64 + lane] = (g[k].x & 0xffffu) | (g[k].z << 16);
        asm volatile("s_waitcnt lgkmcnt(0)" ::: "memory");
        if (lane == 0) __hip_atomic_store(&s_flag, 1u, __ATOMIC_RELAXED, __HIP_MEMORY_SCOPE_WORKGROUP);
        STAMP(3);
    } else if (w == 1) {
        float hv = bflo(b1v);
#pragma unroll
        for (int k = 0; k < 8; ++k) hv += s_hred[k * 16 + (lane & 15)];
        const float u2 = 1.5957691216057308f * (hv + 0.044715f * hv * hv * hv);
        const float th = 1.0f - 2.0f / (__expf(u2) + 1.0f);
        float gl = 0.5f * hv * (1.0f + th);
        if (s >= 14) gl = 0.f;
        if (lane < 16) s_h[lane] = pk_bf16(gl, gl);
    }
    CFENCE();
#pragma unroll
    for (int i = KWIN; i < 16; ++i) {
        if (w != 0) asm volatile("s_waitcnt vmcnt(" STR(KWIN) ")" ::: "memory");
        kr[i] = ldkv(kb + klo + (i >> 2) * 4096 + (i & 3) * 64);
        CFENCE();
    }
#pragma unroll
    for (int i = 0; i < 4; ++i) {
        if (w != 0) asm volatile("s_waitcnt vmcnt(" STR(KWIN) ")" ::: "memory");
        vr[i] = ldkv(vb + vlo + i * 1024);
        CFENCE();
    }
    if (w != 0) {
        for (unsigned spins = 0; __hip_atomic_load(&s_flag, __ATOMIC_RELAXED, __HIP_MEMORY_SCOPE_WORKGROUP) == 0u && spins < (SPIN_CAP << 4); ++spins) __builtin_amdgcn_s_sleep(1);
    }
    CFENCE();
    STAMP(4);
#pragma unroll
    for (int i = 4; i < 16; ++i) vr[i] = ldkv(vb + vlo + i * 1024);
    SB();
    f32x4 S[4][2];
    {
        v4u qf[2][4];
#pragma unroll
        for (int hp = 0; hp < 2; ++hp)
#pragma unroll
            for (int st = 0; st < 4; ++st) qf[hp][st] = *(const v4u*)&s_q[(2 * hp + (r16 >> 3)) * 64 + 16 * st + 4 * rho];
#pragma unroll
        for (int u = 0; u < 4; ++u)
#pragma unroll
            for (int hp = 0; hp < 2; ++hp) {
                f32x4 c = {0.f, 0.f, 0.f, 0.f};
#pragma unroll
                for (int st = 0; st < 4; ++st) c = mfma(kr[u * 4 + st], qf[hp][st], c);
                S[u][hp] = c;
            }
    }
    constexpr float C2 = 0.08838834764831845f * 1.4426950408889634f;
    float mw[2], lw[2];
    unsigned pp[4][2][4];
#pragma unroll
    for (int hp = 0; hp < 2; ++hp) {
        float m = S[0][hp][0];
#pragma unroll
        for (int u = 0; u < 4; ++u)
#pragma unroll
            for (int i = 0; i < 4; ++i) m = fmaxf(m, S[u][hp][i]);
        m = sw16_max(m); m = sw32_max(m);
        m *= C2;
        float l = 0.f;
#pragma unroll
        for (int u = 0; u < 4; ++u)
#pragma unroll
            for (int i = 0; i < 4; ++i) { const float p = __builtin_amdgcn_exp2f(fmaf(S[u][hp][i], C2, -m)); l += p; pp[u][hp][i] = pk_bf16(p, p); }
        l = sw16_add(l, l); l = sw32_add(l, l);
        mw[hp] = m; lw[hp] = l;
    }
    if (lane == 0 || lane == 8) {
        *(f32x2*)&s_ml[(w * 4 + (lane >> 3)) * 2] = f32x2{mw[0], lw[0]};
        *(f32x2*)&s_ml[(w * 4 + 2 + (lane >> 3)) * 2] = f32x2{mw[1], lw[1]};
    }
    SB();
    v4u wor[4], w2r[2];
    {
        const u16* p = wo + (size_t)(kvh * 512 + s * 32) * 512 + 64 * w;
        const unsigned lo = (unsigned)(lane >> 3) * 512u + 8u * (lane & 7);
#pragma unroll
        for (int t = 0; t < 4; ++t) wor[t] = *(const v4u*)(p + lo + t * 8 * 512);
        const u16* p2 = w2 + (size_t)hid0 * 512 + 64 * w;
#pragma unroll
        for (int t = 0; t < 2; ++t) w2r[t] = *(const v4u*)(p2 + lo + t * 8 * 512);
    }
    u64 cfl = __hip_atomic_load((GAS u64*)(ws + WS_FLAGS) + b * 128 + 16 * w + r16, RLX_AGENT);
    SB();
    STAMP(5);
    BAR();
    float fw[8];
    if (w < 2) {
        const int h = tid >> 5;
        float M = s_ml[h * 2];
#pragma unroll
        for (int k = 1; k < 8; ++k) M = fmaxf(M, s_ml[(k * 4 + h) * 2]);
        float Ls = 0.f;
#pragma unroll
        for (int k = 0; k < 8; ++k) { fw[k] = __builtin_amdgcn_exp2f(s_ml[(k * 4 + h) * 2] - M); Ls = fmaf(fw[k], s_ml[(k * 4 + h) * 2 + 1], Ls); }
        if ((tid & 7) == 0) {
            v2u mlv = {min(__float_as_uint(M), SENT - 1u), min(__float_as_uint(Ls), SENT - 1u)};
            __builtin_amdgcn_raw_buffer_store_b64(mlv, rs, WS_BLK + (grp * 16 + (tid >> 3)) * BLK_SZ + BLK_ML + s * 8, 0, AUX_SC1);
        }
    }
    const int j2 = lane & 7;
    unsigned msk[4];
#pragma unroll
    for (int k = 0; k < 4; ++k) msk[k] = (k == (j2 >> 1)) ? ((j2 & 1) ? 0xffff0000u : 0x0000ffffu) : 0u;
    f32x4 O[2] = {f32x4{0.f, 0.f, 0.f, 0.f}, f32x4{0.f, 0.f, 0.f, 0.f}};
#pragma unroll
    for (int u = 0; u < 4; ++u)
#pragma unroll
        for (int i = 0; i < 4; ++i)
#pragma unroll
            for (int hp = 0; hp < 2; ++hp) {
                const unsigned p = pp[u][hp][i];
                O[hp] = mfma(vr[u * 4 + i], v4u{p & msk[0], p & msk[1], p & msk[2], p & msk[3]}, O[hp]);
            }
#pragma unroll
    for (int hp = 0; hp < 2; ++hp) *(f32x4*)&s_ored[(w * 128 + (2 * hp + (r16 >> 3)) * 32 + rho * 8 + j2) * 4] = O[hp];
    STAMP(6);
    BAR();
    if (w < 2) {
        f32x4 acc = {0.f, 0.f, 0.f, 0.f};
#pragma unroll
        for (int k = 0; k < 8; ++k) acc += fw[k] * *(const f32x4*)&s_ored[(k * 128 + tid) * 4];
        __builtin_amdgcn_raw_buffer_store_b128(nosent(acc), rs, WS_BLK + (grp * 16 + (tid >> 3)) * BLK_SZ + BLK_IN1 + s * 128 + (tid & 7) * 16, 0, AUX_SC1);
    }
    STAMP(7);
    f32x4 Y = {0.f, 0.f, 0.f, 0.f};
#pragma unroll
    for (int t = 0; t < 2; ++t) {
        const unsigned p = s_h[(lane >> 3) + 8 * t];
        Y = mfma(w2r[t], v4u{p & msk[0], p & msk[1], p & msk[2], p & msk[3]}, Y);
    }
    for (unsigned spins = 0; !__all(cfl == tag) && spins < SPIN_CAP; ++spins) {
        CFENCE();
        cfl = __hip_atomic_load((GAS u64*)(ws + WS_FLAGS) + b * 128 + 16 * w + r16, RLX_AGENT);
    }
    {
        v4u A, Bv, ML;
        const int base = WS_BLK + L * BLK_SZ;
        for (unsigned spins = 0;; ++spins) {
            CFENCE();
            A = __builtin_amdgcn_raw_buffer_load_b128(rs, base + BLK_IN1 + (2 * j2) * 128 + (lane >> 3) * 16, 0, AUX_SC1);
            Bv = __builtin_amdgcn_raw_buffer_load_b128(rs, base + BLK_IN1 + (2 * j2 + 1) * 128 + (lane >> 3) * 16, 0, AUX_SC1);
            ML = __builtin_amdgcn_raw_buffer_load_b128(rs, base + BLK_ML + j2 * 16, 0, AUX_SC1);
            const bool ok = max(max(max4(A), max4(Bv)), max4(ML)) != SENT;
            if (__all(ok) || spins > SPIN_CAP) break;
        }
        STAMP(8);
        const float M0 = __uint_as_float(ML.x), L0 = __uint_as_float(ML.y), M1 = __uint_as_float(ML.z), L1 = __uint_as_float(ML.w);
        const float Mx = oct_max(fmaxf(M0, M1));
        const float e0 = __builtin_amdgcn_exp2f(M0 - Mx), e1 = __builtin_amdgcn_exp2f(M1 - Mx);
        const float inv = 1.0f / oct_sum(fmaf(e0, L0, e1 * L1));
        const f32x4 o4 = e0 * __builtin_bit_cast(f32x4, A) + e1 * __builtin_bit_cast(f32x4, Bv);
#pragma unroll
        for (int t = 0; t < 4; ++t) {
            const float o = oct_sum(o4[t]) * inv;
            const unsigned p = pk_bf16(o, o);
            Y = mfma(wor[t], v4u{p & msk[0], p & msk[1], p & msk[2], p & msk[3]}, Y);
        }
    }
    {
        f32x4 Z;
#pragma unroll
        for (int i = 0; i < 4; ++i) {
            const float y = dpp<ROR8>(Y[i]);
            auto r = __builtin_amdgcn_permlane32_swap(__float_as_uint(y), __float_as_uint(y), false, false);
            Z[i] = Y[i] + __uint_as_float(r[1]);
        }
        if (lane < 32 && (lane & 8) == 0)
            __builtin_amdgcn_raw_buffer_store_b128(nosent(Z), rs, WS_BLK + (b * 128 + 16 * w + 8 * rho + j2) * BLK_SZ + BLK_IN2 + c2 * 16, 0, AUX_SC1);
    }
    STAMP(9);
    if (w == 7) {
        v4u A, Bv;
        const int base = WS_BLK + L * BLK_SZ + BLK_IN2;
        for (unsigned spins = 0;; ++spins) {
            CFENCE();
            A = __builtin_amdgcn_raw_buffer_load_b128(rs, base + lane * 16, 0, AUX_SC1);
            Bv = __builtin_amdgcn_raw_buffer_load_b128(rs, base + 1024 + lane * 16, 0, AUX_SC1);
            const bool ok = max(max4(A), max4(Bv)) != SENT;
            if (__all(ok) || spins > SPIN_CAP) break;
        }
        STAMP(10);
        const f32x4 v = __builtin_bit_cast(f32x4, A) + __builtin_bit_cast(f32x4, Bv);
        const float t0 = sw32_add(v.x, v.y), t1 = sw32_add(v.z, v.w);
        const float r = row_sum(sw16_add(t0, t1)) + (bflo(xo) + bflo(b2o));
        if (r16 == 0) out[b * 512 + ocol] = (u16)(pk_bf16(r, r) & 0xffffu);
        if (c2 == 0 && lane == 0) __hip_atomic_store((GAS u64*)(ws + WS_EPOCH + b * 256), tag, RLX_AGENT);
        STAMP(11);
    }
}

extern "C" void kernel_launch(void* const* d_in, const int* in_sizes, int n_in, void* d_out, int out_size, void* d_ws, size_t ws_size,
                              hipStream_t stream) {
    fused_decode<<<256, 512, 0, stream>>>((const u16*)d_in[0], (const u16*)d_in[1], (const u16*)d_in[2], (const u16*)d_in[3], (const u16*)d_in[4],
                                          (const u16*)d_in[6], (const u16*)d_in[7], (const u16*)d_in[8], (const u16*)d_in[9], (u16*)d_out,
                                          (unsigned char*)d_ws);
}
```

```cpp
#define PROBE 13
#include <hip/hip_runtime.h>
#include <stdint.h>

typedef unsigned short u16;
typedef unsigned long long u64;
typedef unsigned v4u __attribute__((ext_vector_type(4)));
typedef unsigned v2u __attribute__((ext_vector_type(2)));
typedef float f32x4 __attribute__((ext_vector_type(4)));
typedef float f32x2 __attribute__((ext_vector_type(2)));
typedef __bf16 bf16x8 __attribute__((ext_vector_type(8)));
typedef __bf16 bf16x2 __attribute__((ext_vector_type(2)));
#define GAS __attribute__((address_space(1)))
#define RLX_AGENT __ATOMIC_RELAXED, __HIP_MEMORY_SCOPE_AGENT

#define KWIN 4
#define KV_NT 1

constexpr int WS_EPOCH = 0;
constexpr int WS_FLAGS = 4096;
constexpr int WS_QG = 8192;
constexpr int WS_BLK = 131072;
constexpr int BLK_SZ = 4608, BLK_IN1 = 0, BLK_IN2 = 2048, BLK_ML = 4096;
constexpr int WS_BYTES = WS_BLK + 256 * BLK_SZ;
constexpr int WS_STAMP = 16 << 20;
constexpr unsigned SENT = 0xFFFFFFFFu;
constexpr int AUX_SC1 = 16;
constexpr unsigned SPIN_CAP = 1u << 18;

template <int CTRL> __device__ __forceinline__ float dpp(float x) {
    return __builtin_bit_cast(float, __builtin_amdgcn_mov_dpp(__builtin_bit_cast(int, x), CTRL, 0xf, 0xf, true));
}
constexpr int XOR1 = 0xB1, XOR2 = 0x4E, HMIR = 0x141, ROR8 = 0x128, ROR4 = 0x124, ROR2 = 0x122;
__device__ __forceinline__ float sw32_add(float a, float b) {
    auto r = __builtin_amdgcn_permlane32_swap(__float_as_uint(a), __float_as_uint(b), false, false);
    return __uint_as_float(r[0]) + __uint_as_float(r[1]);
}
__device__ __forceinline__ float sw16_add(float a, float b) {
    auto r = __builtin_amdgcn_permlane16_swap(__float_as_uint(a), __float_as_uint(b), false, false);
    return __uint_as_float(r[0]) + __uint_as_float(r[1]);
}
__device__ __forceinline__ float sw32_max(float a) {
    auto r = __builtin_amdgcn_permlane32_swap(__float_as_uint(a), __float_as_uint(a), false, false);
    return fmaxf(__uint_as_float(r[0]), __uint_as_float(r[1]));
}
__device__ __forceinline__ float sw16_max(float a) {
    auto r = __builtin_amdgcn_permlane16_swap(__float_as_uint(a), __float_as_uint(a), false, false);
    return fmaxf(__uint_as_float(r[0]), __uint_as_float(r[1]));
}
__device__ __forceinline__ float row_sum(float x) { x += dpp<XOR1>(x); x += dpp<XOR2>(x); x += dpp<HMIR>(x); x += dpp<ROR8>(x); return x; }
__device__ __forceinline__ float oct_sum(float x) { x += dpp<XOR1>(x); x += dpp<XOR2>(x); x += dpp<HMIR>(x); return x; }
__device__ __forceinline__ float oct_max(float x) { x = fmaxf(x, dpp<XOR1>(x)); x = fmaxf(x, dpp<XOR2>(x)); x = fmaxf(x, dpp<HMIR>(x)); return x; }
__device__ __forceinline__ float wave_sum(float x) { x = row_sum(x); x = sw16_add(x, x); x = sw32_add(x, x); return x; }
__device__ __forceinline__ float bflo(unsigned u) { return __uint_as_float(u << 16); }
__device__ __forceinline__ float bfhi(unsigned u) { return __uint_as_float(u & 0xffff0000u); }
__device__ __forceinline__ unsigned pk_bf16(float lo, float hi) {
    f32x2 v = {lo, hi};
    return __builtin_bit_cast(unsigned, __builtin_convertvector(v, bf16x2));
}
__device__ __forceinline__ float rbf(float x) { return bflo(pk_bf16(x, x)); }
__device__ __forceinline__ f32x4 mfma(v4u a, v4u b, f32x4 c) {
    return __builtin_amdgcn_mfma_f32_16x16x32_bf16(__builtin_bit_cast(bf16x8, a), __builtin_bit_cast(bf16x8, b), c, 0, 0, 0);
}
__device__ __forceinline__ v4u ldkv(const unsigned char* p) {
    return __builtin_nontemporal_load((const v4u*)p);
}
__device__ __forceinline__ v4u nosent(f32x4 v) {
    v4u u = __builtin_bit_cast(v4u, v);
    u.x = min(u.x, SENT - 1u); u.y = min(u.y, SENT - 1u); u.z = min(u.z, SENT - 1u); u.w = min(u.w, SENT - 1u);
    return u;
}
__device__ __forceinline__ unsigned max4(v4u a) { return max(max(a.x, a.y), max(a.z, a.w)); }
#define SB() __builtin_amdgcn_sched_barrier(0)
#define CFENCE() asm volatile("" ::: "memory")
#define BAR() do { asm volatile("s_waitcnt lgkmcnt(0)" ::: "memory"); __builtin_amdgcn_s_barrier(); asm volatile("" ::: "memory"); } while (0)
#define STR2(x) #x
#define STR(x) STR2(x)

#define STAMP(i) do { if ((i) == PROBE) pt1 = __builtin_amdgcn_s_memrealtime(); } while (0)

struct KA { const u16* p[9]; u16* out; unsigned char* ws; };
__global__ void __launch_bounds__(512, 2)
fused_decode(const u16* x_, const u16* Kc_, const u16* Vc_, const u16* wq_, const u16* wo_, const u16* w1_, const u16* b1_, const u16* w2_,
             const u16* b2_, u16* out_, unsigned char* ws_) {
    u64 pt0 = 0, pt1 = 0;
    const __attribute__((address_space(4))) KA* ka = (const __attribute__((address_space(4))) KA*)__builtin_amdgcn_kernarg_segment_ptr();
    asm volatile("s_memrealtime %0\n\ts_waitcnt lgkmcnt(0)" : "=s"(pt0), "+s"(ka) :: "memory");
    const u16* __restrict__ x = ka->p[0]; const u16* __restrict__ Kc = ka->p[1]; const u16* __restrict__ Vc = ka->p[2]; const u16* __restrict__ wq = ka->p[3];
    const u16* __restrict__ wo = ka->p[4]; const u16* __restrict__ w1 = ka->p[5]; const u16* __restrict__ b1 = ka->p[6]; const u16* __restrict__ w2 = ka->p[7];
    const u16* __restrict__ b2 = ka->p[8]; u16* __restrict__ out = ka->out; unsigned char* __restrict__ ws = ka->ws;
    asm volatile("" :: "s"(x), "s"(Kc), "s"(Vc), "s"(wq), "s"(wo), "s"(w1), "s"(b1), "s"(w2), "s"(b2), "s"(out), "s"(ws));
    if (PROBE == 13) asm volatile("s_memrealtime %0\n\ts_waitcnt lgkmcnt(0)" : "=s"(pt1) :: "memory");
    __shared__ __attribute__((aligned(16))) float s_ored[8 * 128 * 4];
    __shared__ __attribute__((aligned(16))) float s_qred[8 * 32];
    __shared__ __attribute__((aligned(16))) float s_hred[8 * 16];
    __shared__ __attribute__((aligned(16))) unsigned s_q[256];
    __shared__ __attribute__((aligned(16))) unsigned s_h[16];
    __shared__ __attribute__((aligned(16))) float s_ml[8 * 4 * 2];
    __shared__ unsigned s_flag;

    const int bid = blockIdx.x, kvh = bid & 7, rr = bid >> 3, b = rr >> 4, s = rr & 15;
    const int tid = threadIdx.x, lane = tid & 63, w = __builtin_amdgcn_readfirstlane(tid >> 6);
    const int L = b * 128 + kvh * 16 + s, grp = b * 8 + kvh;
    const __amdgpu_buffer_rsrc_t rs = __builtin_amdgcn_make_buffer_rsrc(ws, 0, WS_BYTES, 0x00020000);
    STAMP(0);
    if (tid < 264) __builtin_amdgcn_raw_buffer_store_b128(v4u{SENT, SENT, SENT, SENT}, rs, WS_BLK + L * BLK_SZ + tid * 16, 0, AUX_SC1);
    if (tid == 0) s_flag = 0;
    CFENCE();
    const u64 epoch = __hip_atomic_load((GAS u64*)(ws + WS_EPOCH + b * 256), RLX_AGENT);
    const u16* xb = x + b * 512;
    const v4u xa = *(const v4u*)(xb + 8 * lane);
    const v2u xq2 = *(const v2u*)(xb + 64 * w + 4 * (lane >> 2));
    const unsigned xm2 = *(const unsigned*)(xb + 64 * w + 2 * (lane >> 1));
    v4u wqr[4];
    {
        const u16* p = wq + (size_t)(64 * w) * 4096 + kvh * 512 + s * 32;
        const unsigned lo = (unsigned)(4 * (lane >> 2)) * 4096u + 8u * (lane & 3);
#pragma unroll
        for (int i = 0; i < 4; ++i) wqr[i] = *(const v4u*)(p + lo + i * 4096);
    }
    const int sm = s < 14 ? s : s - 2;
    const int hid0 = kvh * 224 + sm * 16;
    v4u w1r[2];
    {
        const u16* p = w1 + (size_t)(64 * w) * 1792 + hid0;
        const unsigned lo = (unsigned)(2 * (lane >> 1)) * 1792u + 8u * (lane & 1);
#pragma unroll
        for (int i = 0; i < 2; ++i) w1r[i] = *(const v4u*)(p + lo + i * 1792);
    }
    const unsigned b1v = b1[hid0 + (lane & 15)];
    const int c2 = kvh * 16 + s;
    const int rho = lane >> 4;
    const int ocol = 64 * (c2 >> 4) + 32 * ((c2 >> 3) & 1) + 8 * (2 * (rho & 1) + (rho >> 1)) + (c2 & 7);
    const unsigned xo = xb[ocol], b2o = b2[ocol];
    SB();
    const size_t kvoff = ((size_t)(b * 8 + kvh) * 8192 + s * 512 + w * 64) * 256;
    const unsigned char* kb = (const unsigned char*)Kc + kvoff;
    const unsigned char* vb = (const unsigned char*)Vc + kvoff;
    const unsigned r16 = lane & 15;
    const unsigned klo = (4u * (r16 & 3) + (r16 >> 2)) * 256u + 16u * (lane >> 4);
    const unsigned vlo = 16u * lane;
    v4u kr[16], vr[16];
#pragma unroll
    for (int i = 0; i < KWIN; ++i) kr[i] = ldkv(kb + klo + (i >> 2) * 4096 + (i & 3) * 64);
    SB();
    float xs[8];
#pragma unroll
    for (int k = 0; k < 4; ++k) { xs[2 * k] = bflo(xa[k]); xs[2 * k + 1] = bfhi(xa[k]); }
    float sum = ((xs[0] + xs[1]) + (xs[2] + xs[3])) + ((xs[4] + xs[5]) + (xs[6] + xs[7]));
    const float mean = wave_sum(sum) * (1.0f / 512.0f);
    float sq = 0.f;
#pragma unroll
    for (int k = 0; k < 8; ++k) { const float d = xs[k] - mean; sq = fmaf(d, d, sq); }
    const float var = wave_sum(sq) * (1.0f / 512.0f);
    const float rstd = 1.0f / sqrtf(var + 1e-5f);
    {
        float xn[4] = {rbf((bflo(xq2.x) - mean) * rstd), rbf((bfhi(xq2.x) - mean) * rstd), rbf((bflo(xq2.y) - mean) * rstd), rbf((bfhi(xq2.y) - mean) * rstd)};
        float a[8];
#pragma unroll
        for (int k = 0; k < 8; ++k) a[k] = 0.f;
#pragma unroll
        for (int i = 0; i < 4; ++i)
#pragma unroll
            for (int k = 0; k < 4; ++k) { a[2 * k] = fmaf(xn[i], bflo(wqr[i][k]), a[2 * k]); a[2 * k + 1] = fmaf(xn[i], bfhi(wqr[i][k]), a[2 * k + 1]); }
        float t[4], u[2];
#pragma unroll
        for (int j = 0; j < 4; ++j) t[j] = sw32_add(a[j], a[j + 4]);
#pragma unroll
        for (int j = 0; j < 2; ++j) { u[j] = sw16_add(t[j], t[j + 2]); u[j] += dpp<ROR8>(u[j]); u[j] += dpp<ROR4>(u[j]); }
        if (r16 < 4) *(f32x2*)&s_qred[w * 32 + 8 * r16 + 2 * (rho & 1) + 4 * (rho >> 1)] = f32x2{u[0], u[1]};
    }
    {
        float xn[2] = {rbf((bflo(xm2) - mean) * rstd), rbf((bfhi(xm2) - mean) * rstd)};
        float a[8];
#pragma unroll
        for (int k = 0; k < 8; ++k) a[k] = 0.f;
#pragma unroll
        for (int i = 0; i < 2; ++i)
#pragma unroll
            for (int k = 0; k < 4; ++k) { a[2 * k] = fmaf(xn[i], bflo(w1r[i][k]), a[2 * k]); a[2 * k + 1] = fmaf(xn[i], bfhi(w1r[i][k]), a[2 * k + 1]); }
        float t[4], u[2];
#pragma unroll
        for (int j = 0; j < 4; ++j) t[j] = sw32_add(a[j], a[j + 4]);
#pragma unroll
        for (int j = 0; j < 2; ++j) { u[j] = sw16_add(t[j], t[j + 2]); u[j] += dpp<ROR8>(u[j]); u[j] += dpp<ROR4>(u[j]); u[j] += dpp<ROR2>(u[j]); }
        if (r16 < 2) *(f32x2*)&s_hred[w * 16 + 8 * r16 + 2 * (rho & 1) + 4 * (rho >> 1)] = f32x2{u[0], u[1]};
    }
    STAMP(1);
    BAR();
    const u64 tag = epoch + 1ull;
    if (w == 0) {
        float qv = 0.f;
#pragma unroll
        for (int k = 0; k < 8; ++k) qv += s_qred[k * 32 + (lane & 31)];
        const u64 gran = (tag << 16) | (u64)(pk_bf16(qv, qv) & 0xffffu);
        if (lane < 32) __hip_atomic_store((GAS u64*)(ws + WS_QG) + grp * 512 + s * 32 + lane, gran, RLX_AGENT);
        else if (lane == 32) __hip_atomic_store((GAS u64*)(ws + WS_FLAGS) + L, tag, RLX_AGENT);
        STAMP(2);
        const unsigned thi = (unsigned)(tag >> 16), tlo = (unsigned)(tag & 0xffffu);
        v4u g[4];
        for (unsigned spins = 0;; ++spins) {
            CFENCE();
#pragma unroll
            for (int k = 0; k < 4; ++k) g[k] = __builtin_amdgcn_raw_buffer_load_b128(rs, WS_QG + grp * 4096 + k * 1024 + lane * 16, 0, AUX_SC1);
            bool ok = true;
#pragma unroll
            for (int k = 0; k < 4; ++k) ok = ok && g[k].y == thi && g[k].w == thi && (g[k].x >> 16) == tlo && (g[k].z >> 16) == tlo;
            if (__all(ok) || spins > SPIN_CAP) break;
        }
#pragma unroll
        for (int k = 0; k < 4; ++k) s_q[k * 64 + lane] = (g[k].x & 0xffffu) | (g[k].z << 16);
        asm volatile("s_waitcnt lgkmcnt(0)" ::: "memory");
        if (lane == 0) __hip_atomic_store(&s_flag, 1u, __ATOMIC_RELAXED, __HIP_MEMORY_SCOPE_WORKGROUP);
        STAMP(3);
    } else if (w == 1) {
        float hv = bflo(b1v);
#pragma unroll
        for (int k = 0; k < 8; ++k) hv += s_hred[k * 16 + (lane & 15)];
        const float u2 = 1.5957691216057308f * (hv + 0.044715f * hv * hv * hv);
        const float th = 1.0f - 2.0f / (__expf(u2) + 1.0f);
        float gl = 0.5f * hv * (1.0f + th);
        if (s >= 14) gl = 0.f;
        if (lane < 16) s_h[lane] = pk_bf16(gl, gl);
    }
    CFENCE();
#pragma unroll
    for (int i = KWIN; i < 16; ++i) {
        if (w != 0) asm volatile("s_waitcnt vmcnt(" STR(KWIN) ")" ::: "memory");
        kr[i] = ldkv(kb + klo + (i >> 2) * 4096 + (i & 3) * 64);
        CFENCE();
    }
#pragma unroll
    for (int i = 0; i < 4; ++i) {
        if (w != 0) asm volatile("s_waitcnt vmcnt(" STR(KWIN) ")" ::: "memory");
        vr[i] = ldkv(vb + vlo + i * 1024);
        CFENCE();
    }
    if (w != 0) {
        for (unsigned spins = 0; __hip_atomic_load(&s_flag, __ATOMIC_RELAXED, __HIP_MEMORY_SCOPE_WORKGROUP) == 0u && spins < (SPIN_CAP << 4); ++spins) __builtin_amdgcn_s_sleep(1);
    }
    CFENCE();
    STAMP(4);
#pragma unroll
    for (int i = 4; i < 16; ++i) vr[i] = ldkv(vb + vlo + i * 1024);
    SB();
    f32x4 S[4][2];
    {
        v4u qf[2][4];
#pragma unroll
        for (int hp = 0; hp < 2; ++hp)
#pragma unroll
            for (int st = 0; st < 4; ++st) qf[hp][st] = *(const v4u*)&s_q[(2 * hp + (r16 >> 3)) * 64 + 16 * st + 4 * rho];
#pragma unroll
        for (int u = 0; u < 4; ++u)
#pragma unroll
            for (int hp = 0; hp < 2; ++hp) {
                f32x4 c = {0.f, 0.f, 0.f, 0.f};
#pragma unroll
                for (int st = 0; st < 4; ++st) c = mfma(kr[u * 4 + st], qf[hp][st], c);
                S[u][hp] = c;
            }
    }
    constexpr float C2 = 0.08838834764831845f * 1.4426950408889634f;
    float mw[2], lw[2];
    unsigned pp[4][2][4];
#pragma unroll
    for (int hp = 0; hp < 2; ++hp) {
        float m = S[0][hp][0];
#pragma unroll
        for (int u = 0; u < 4; ++u)
#pragma unroll
            for (int i = 0; i < 4; ++i) m = fmaxf(m, S[u][hp][i]);
        m = sw16_max(m); m = sw32_max(m);
        m *= C2;
        float l = 0.f;
#pragma unroll
        for (int u = 0; u < 4; ++u)
#pragma unroll
            for (int i = 0; i < 4; ++i) { const float p = __builtin_amdgcn_exp2f(fmaf(S[u][hp][i], C2, -m)); l += p; pp[u][hp][i] = pk_bf16(p, p); }
        l = sw16_add(l, l); l = sw32_add(l, l);
        mw[hp] = m; lw[hp] = l;
    }
    if (lane == 0 || lane == 8) {
        *(f32x2*)&s_ml[(w * 4 + (lane >> 3)) * 2] = f32x2{mw[0], lw[0]};
        *(f32x2*)&s_ml[(w * 4 + 2 + (lane >> 3)) * 2] = f32x2{mw[1], lw[1]};
    }
    SB();
    v4u wor[4], w2r[2];
    {
        const u16* p = wo + (size_t)(kvh * 512 + s * 32) * 512 + 64 * w;
        const unsigned lo = (unsigned)(lane >> 3) * 512u + 8u * (lane & 7);
#pragma unroll
        for (int t = 0; t < 4; ++t) wor[t] = *(const v4u*)(p + lo + t * 8 * 512);
        const u16* p2 = w2 + (size_t)hid0 * 512 + 64 * w;
#pragma unroll
        for (int t = 0; t < 2; ++t) w2r[t] = *(const v4u*)(p2 + lo + t * 8 * 512);
    }
    u64 cfl = __hip_atomic_load((GAS u64*)(ws + WS_FLAGS) + b * 128 + 16 * w + r16, RLX_AGENT);
    SB();
    STAMP(5);
    BAR();
    float fw[8];
    if (w < 2) {
        const int h = tid >> 5;
        float M = s_ml[h * 2];
#pragma unroll
        for (int k = 1; k < 8; ++k) M = fmaxf(M, s_ml[(k * 4 + h) * 2]);
        float Ls = 0.f;
#pragma unroll
        for (int k = 0; k < 8; ++k) { fw[k] = __builtin_amdgcn_exp2f(s_ml[(k * 4 + h) * 2] - M); Ls = fmaf(fw[k], s_ml[(k * 4 + h) * 2 + 1], Ls); }
        if ((tid & 7) == 0) {
            v2u mlv = {min(__float_as_uint(M), SENT - 1u), min(__float_as_uint(Ls), SENT - 1u)};
            __builtin_amdgcn_raw_buffer_store_b64(mlv, rs, WS_BLK + (grp * 16 + (tid >> 3)) * BLK_SZ + BLK_ML + s * 8, 0, AUX_SC1);
        }
    }
    const int j2 = lane & 7;
    unsigned msk[4];
#pragma unroll
    for (int k = 0; k < 4; ++k) msk[k] = (k == (j2 >> 1)) ? ((j2 & 1) ? 0xffff0000u : 0x0000ffffu) : 0u;
    f32x4 O[2] = {f32x4{0.f, 0.f, 0.f, 0.f}, f32x4{0.f, 0.f, 0.f, 0.f}};
#pragma unroll
    for (int u = 0; u < 4; ++u)
#pragma unroll
        for (int i = 0; i < 4; ++i)
#pragma unroll
            for (int hp = 0; hp < 2; ++hp) {
                const unsigned p = pp[u][hp][i];
                O[hp] = mfma(vr[u * 4 + i], v4u{p & msk[0], p & msk[1], p & msk[2], p & msk[3]}, O[hp]);
            }
#pragma unroll
    for (int hp = 0; hp < 2; ++hp) *(f32x4*)&s_ored[(w * 128 + (2 * hp + (r16 >> 3)) * 32 + rho * 8 + j2) * 4] = O[hp];
    STAMP(6);
    BAR();
    if (w < 2) {
        f32x4 acc = {0.f, 0.f, 0.f, 0.f};
#pragma unroll
        for (int k = 0; k < 8; ++k) acc += fw[k] * *(const f32x4*)&s_ored[(k * 128 + tid) * 4];
        __builtin_amdgcn_raw_buffer_store_b128(nosent(acc), rs, WS_BLK + (grp * 16 + (tid >> 3)) * BLK_SZ + BLK_IN1 + s * 128 + (tid & 7) * 16, 0, AUX_SC1);
    }
    STAMP(7);
    f32x4 Y = {0.f, 0.f, 0.f, 0.f};
#pragma unroll
    for (int t = 0; t < 2; ++t) {
        const unsigned p = s_h[(lane >> 3) + 8 * t];
        Y = mfma(w2r[t], v4u{p & msk[0], p & msk[1], p & msk[2], p & msk[3]}, Y);
    }
    for (unsigned spins = 0; !__all(cfl == tag) && spins < SPIN_CAP; ++spins) {
        CFENCE();
        cfl = __hip_atomic_load((GAS u64*)(ws + WS_FLAGS) + b * 128 + 16 * w + r16, RLX_AGENT);
    }
    {
        v4u A, Bv, ML;
        const int base = WS_BLK + L * BLK_SZ;
        for (unsigned spins = 0;; ++spins) {
            CFENCE();
            A = __builtin_amdgcn_raw_buffer_load_b128(rs, base + BLK_IN1 + (2 * j2) * 128 + (lane >> 3) * 16, 0, AUX_SC1);
            Bv = __builtin_amdgcn_raw_buffer_load_b128(rs, base + BLK_IN1 + (2 * j2 + 1) * 128 + (lane >> 3) * 16, 0, AUX_SC1);
            ML = __builtin_amdgcn_raw_buffer_load_b128(rs, base + BLK_ML + j2 * 16, 0, AUX_SC1);
            const bool ok = max(max(max4(A), max4(Bv)), max4(ML)) != SENT;
            if (__all(ok) || spins > SPIN_CAP) break;
        }
        STAMP(8);
        const float M0 = __uint_as_float(ML.x), L0 = __uint_as_float(ML.y), M1 = __uint_as_float(ML.z), L1 = __uint_as_float(ML.w);
        const float Mx = oct_max(fmaxf(M0, M1));
        const float e0 = __builtin_amdgcn_exp2f(M0 - Mx), e1 = __builtin_amdgcn_exp2f(M1 - Mx);
        const float inv = 1.0f / oct_sum(fmaf(e0, L0, e1 * L1));
        const f32x4 o4 = e0 * __builtin_bit_cast(f32x4, A) + e1 * __builtin_bit_cast(f32x4, Bv);
#pragma unroll
        for (int t = 0; t < 4; ++t) {
            const float o = oct_sum(o4[t]) * inv;
            const unsigned p = pk_bf16(o, o);
            Y = mfma(wor[t], v4u{p & msk[0], p & msk[1], p & msk[2], p & msk[3]}, Y);
        }
    }
    {
        f32x4 Z;
#pragma unroll
        for (int i = 0; i < 4; ++i) {
            const float y = dpp<ROR8>(Y[i]);
            auto r = __builtin_amdgcn_permlane32_swap(__float_as_uint(y), __float_as_uint(y), false, false);
            Z[i] = Y[i] + __uint_as_float(r[1]);
        }
        if (lane < 32 && (lane & 8) == 0)
            __builtin_amdgcn_raw_buffer_store_b128(nosent(Z), rs, WS_BLK + (b * 128 + 16 * w + 8 * rho + j2) * BLK_SZ + BLK_IN2 + c2 * 16, 0, AUX_SC1);
    }
    STAMP(9);
    if (w == 7) {
        v4u A, Bv;
        const int base = WS_BLK + L * BLK_SZ + BLK_IN2;
        for (unsigned spins = 0;; ++spins) {
            CFENCE();
            A = __builtin_amdgcn_raw_buffer_load_b128(rs, base + lane * 16, 0, AUX_SC1);
            Bv = __builtin_amdgcn_raw_buffer_load_b128(rs, base + 1024 + lane * 16, 0, AUX_SC1);
            const bool ok = max(max4(A), max4(Bv)) != SENT;
            if (__all(ok) || spins > SPIN_CAP) break;
        }
        STAMP(10);
        const f32x4 v = __builtin_bit_cast(f32x4, A) + __builtin_bit_cast(f32x4, Bv);
        const float t0 = sw32_add(v.x, v.y), t1 = sw32_add(v.z, v.w);
        const float r = row_sum(sw16_add(t0, t1)) + (bflo(xo) + bflo(b2o));
        if (r16 == 0) out[b * 512 + ocol] = (u16)(pk_bf16(r, r) & 0xffffu);
        if (c2 == 0 && lane == 0) __hip_atomic_store((GAS u64*)(ws + WS_EPOCH + b * 256), tag, RLX_AGENT);
        STAMP(11);
        { const u64 until = __builtin_amdgcn_s_memrealtime() + (PROBE == 99 ? 0 : (pt1 - pt0)); while (__builtin_amdgcn_s_memrealtime() < until) __builtin_amdgcn_s_sleep(1); }
    }
}

extern "C" void kernel_launch(void* const* d_in, const int* in_sizes, int n_in, void* d_out, int out_size, void* d_ws, size_t ws_size,
                              hipStream_t stream) {
    fused_decode<<<256, 512, 0, stream>>>((const u16*)d_in[0], (const u16*)d_in[1], (const u16*)d_in[2], (const u16*)d_in[3], (const u16*)d_in[4],
                                          (const u16*)d_in[6], (const u16*)d_in[7], (const u16*)d_in[8], (const u16*)d_in[9], (u16*)d_out,
                                          (unsigned char*)d_ws);
}
```

```cpp
#include <hip/hip_runtime.h>
#include <stdint.h>

typedef unsigned short u16;
typedef unsigned long long u64;
typedef unsigned v4u __attribute__((ext_vector_type(4)));
typedef unsigned v2u __attribute__((ext_vector_type(2)));
typedef float f32x4 __attribute__((ext_vector_type(4)));
typedef float f32x2 __attribute__((ext_vector_type(2)));
typedef short s16x4 __attribute__((ext_vector_type(4)));
typedef __bf16 bf16x8 __attribute__((ext_vector_type(8)));
typedef __bf16 bf16x2 __attribute__((ext_vector_type(2)));
#define GAS __attribute__((address_space(1)))
#define RLX_AGENT __ATOMIC_RELAXED, __HIP_MEMORY_SCOPE_AGENT
#define RLX_WG __ATOMIC_RELAXED, __HIP_MEMORY_SCOPE_WORKGROUP

constexpr int K0 = 5;
constexpr int WS_EPOCH = 0;
constexpr int WS_FLAGS = 4096;
constexpr int WS_QG = 8192;
constexpr int WS_BLK = 131072;
constexpr int BLK_SZ = 3072, BLK_IN1 = 0, BLK_ML = 2048;
constexpr int WS_IN2 = WS_BLK + 256 * BLK_SZ;
constexpr int WS_QA = WS_IN2 + 32 * 16384;
constexpr int WS_HG = WS_QA + 64 * 4096;
constexpr int WS_BYTES = WS_HG + 2 * 128 * 1024;
constexpr unsigned SENT = 0xFFFFFFFFu;
constexpr int AUX_SC1 = 16;
constexpr unsigned SPIN_CAP = 1u << 18;

template <int CTRL> __device__ __forceinline__ float dpp(float x) {
    return __builtin_bit_cast(float, __builtin_amdgcn_mov_dpp(__builtin_bit_cast(int, x), CTRL, 0xf, 0xf, true));
}
constexpr int XOR1 = 0xB1, XOR2 = 0x4E, HMIR = 0x141, ROR8 = 0x128, BC0 = 0x00, BC2 = 0xAA;
__device__ __forceinline__ float sw32_add(float a, float b) {
    auto r = __builtin_amdgcn_permlane32_swap(__float_as_uint(a), __float_as_uint(b), false, false);
    return __uint_as_float(r[0]) + __uint_as_float(r[1]);
}
__device__ __forceinline__ float sw16_add(float a, float b) {
    auto r = __builtin_amdgcn_permlane16_swap(__float_as_uint(a), __float_as_uint(b), false, false);
    return __uint_as_float(r[0]) + __uint_as_float(r[1]);
}
__device__ __forceinline__ float sw32_max(float a) {
    auto r = __builtin_amdgcn_permlane32_swap(__float_as_uint(a), __float_as_uint(a), false, false);
    return fmaxf(__uint_as_float(r[0]), __uint_as_float(r[1]));
}
__device__ __forceinline__ float sw16_max(float a) {
    auto r = __builtin_amdgcn_permlane16_swap(__float_as_uint(a), __float_as_uint(a), false, false);
    return fmaxf(__uint_as_float(r[0]), __uint_as_float(r[1]));
}
__device__ __forceinline__ float oct_sum(float x) { x += dpp<XOR1>(x); x += dpp<XOR2>(x); x += dpp<HMIR>(x); return x; }
__device__ __forceinline__ float oct_max(float x) { x = fmaxf(x, dpp<XOR1>(x)); x = fmaxf(x, dpp<XOR2>(x)); x = fmaxf(x, dpp<HMIR>(x)); return x; }
__device__ __forceinline__ float row_sum(float x) { x = oct_sum(x); x += dpp<ROR8>(x); return x; }
__device__ __forceinline__ float bflo(unsigned u) { return __uint_as_float(u << 16); }
__device__ __forceinline__ float bfhi(unsigned u) { return __uint_as_float(u & 0xffff0000u); }
__device__ __forceinline__ unsigned pk_bf16(float lo, float hi) {
    f32x2 v = {lo, hi};
    return __builtin_bit_cast(unsigned, __builtin_convertvector(v, bf16x2));
}
__device__ __forceinline__ float dot2(unsigned a, unsigned b, float c) {
    return __builtin_amdgcn_fdot2_f32_bf16(__builtin_bit_cast(bf16x2, a), __builtin_bit_cast(bf16x2, b), c, false);
}
__device__ __forceinline__ f32x4 mfma32(v4u a, v4u b, f32x4 c) {
    return __builtin_amdgcn_mfma_f32_16x16x32_bf16(__builtin_bit_cast(bf16x8, a), __builtin_bit_cast(bf16x8, b), c, 0, 0, 0);
}
__device__ __forceinline__ f32x4 mfma16(unsigned a0, unsigned a1, unsigned b0, unsigned b1, f32x4 c) {
    return __builtin_amdgcn_mfma_f32_16x16x16bf16_1k(__builtin_bit_cast(s16x4, v2u{a0, a1}), __builtin_bit_cast(s16x4, v2u{b0, b1}), c, 0, 0, 0);
}
__device__ __forceinline__ v4u ld_nt(const unsigned char* p) { return __builtin_nontemporal_load((const v4u*)p); }
__device__ __forceinline__ v4u nosent(f32x4 v) {
    v4u u = __builtin_bit_cast(v4u, v);
    u.x = min(u.x, SENT - 1u); u.y = min(u.y, SENT - 1u); u.z = min(u.z, SENT - 1u); u.w = min(u.w, SENT - 1u);
    return u;
}
__device__ __forceinline__ unsigned max4(v4u a) { return max(max(a.x, a.y), max(a.z, a.w)); }

#define POLL_CHECK(b0, b1, b2, b3, b4, b5, b6, b7) \
    "v_max3_u32 v144, v" #b0 ", v" #b1 ", v" #b2 "\n\tv_max3_u32 v145, v" #b3 ", v" #b4 ", v" #b5 "\n\t" \
    "v_max3_u32 v144, v144, v" #b6 ", v" #b7 "\n\tv_max_u32 v144, v144, v145\n\tv_cmp_eq_u32 vcc, -1, v144\n\t" \
    "v_mul_f32 %0, %11, v" #b0 "\n\tv_mul_f32 %1, %11, v" #b1 "\n\tv_mul_f32 %2, %11, v" #b2 "\n\tv_mul_f32 %3, %11, v" #b3 "\n\t" \
    "v_fmac_f32 %0, %12, v" #b4 "\n\tv_fmac_f32 %1, %12, v" #b5 "\n\tv_fmac_f32 %2, %12, v" #b6 "\n\tv_fmac_f32 %3, %12, v" #b7 "\n\t" \
    "s_mov_b64 exec, vcc\n\t" \
    "s_cbranch_vccz 2f\n\t"
#define POLL_PAIR(D, r, k0, k1, k2, k3, off, srd, e0, e1) do { unsigned cnt_; u64 ex_; \
    asm volatile( \
        "s_mov_b64 %9, exec\n\t" \
        "buffer_load_dwordx4 v[128:131], %10, %13, 0 offen sc1\n\t" \
        "buffer_load_dwordx4 v[132:135], %10, %13, 0 offen offset:" #D " sc1\n\t" \
        "s_mov_b32 %4, 0x40000\n\t" \
        "s_sleep 8\n" \
        "1:\n\t" \
        "buffer_load_dwordx4 v[136:139], %10, %13, 0 offen sc1\n\t" \
        "buffer_load_dwordx4 v[140:143], %10, %13, 0 offen offset:" #D " sc1\n\t" \
        "s_waitcnt vmcnt(2)\n\t" \
        POLL_CHECK(128, 129, 130, 131, 132, 133, 134, 135) \
        "buffer_load_dwordx4 v[128:131], %10, %13, 0 offen sc1\n\t" \
        "buffer_load_dwordx4 v[132:135], %10, %13, 0 offen offset:" #D " sc1\n\t" \
        "s_waitcnt vmcnt(2)\n\t" \
        POLL_CHECK(136, 137, 138, 139, 140, 141, 142, 143) \
        "s_sub_u32 %4, %4, 1\n\t" \
        "s_cbranch_scc0 1b\n" \
        "2:\n\t" \
        "s_mov_b64 exec, %9\n\t" \
        "s_nop 4" \
        : "=&v"(r[0]), "=&v"(r[1]), "=&v"(r[2]), "=&v"(r[3]), "=&s"(cnt_), \
          "=&{v[128:131]}"(k0), "=&{v[132:135]}"(k1), "=&{v[136:139]}"(k2), "=&{v[140:143]}"(k3), "=&s"(ex_) \
        : "v"(off), "v"(e0), "v"(e1), "s"(srd) \
        : "v144", "v145", "vcc", "scc", "memory"); } while (0)
#define POLL_DRAIN(k0, k1, k2, k3) \
    asm volatile("s_waitcnt vmcnt(0)" :: "{v[128:131]}"(k0), "{v[132:135]}"(k1), "{v[136:139]}"(k2), "{v[140:143]}"(k3) : "memory")
#define SB() __builtin_amdgcn_sched_barrier(0)
#define CFENCE() asm volatile("" ::: "memory")
#define LGKM0() asm volatile("s_waitcnt lgkmcnt(0)" ::: "memory")
#define BAR() do { asm volatile("s_waitcnt lgkmcnt(0)" ::: "memory"); __builtin_amdgcn_s_barrier(); asm volatile("" ::: "memory"); } while (0)
#define LDS_WAIT(word, target, slp) \
    for (unsigned spins_ = 0; __hip_atomic_load(&(word), RLX_WG) < (target) && spins_ < (SPIN_CAP << 4); ++spins_) __builtin_amdgcn_s_sleep(slp)
#define LD_SC1(off) __builtin_amdgcn_raw_buffer_load_b128(rs, (off), 0, AUX_SC1)
#define ST_SC1(v, off) __builtin_amdgcn_raw_buffer_store_b128((v), rs, (off), 0, AUX_SC1)

__global__ void __launch_bounds__(576, 3)
fused_decode(const u16* __restrict__ x, const u16* __restrict__ Kc, const u16* __restrict__ Vc, const u16* __restrict__ wq,
             const u16* __restrict__ wo, const u16* __restrict__ w1, const u16* __restrict__ b1, const u16* __restrict__ w2,
             const u16* __restrict__ b2, u16* __restrict__ out, unsigned char* __restrict__ ws) {
    __shared__ __attribute__((aligned(16))) float s_ored[8 * 128 * 4];
    __shared__ __attribute__((aligned(16))) float s_qred[8 * 128];
    __shared__ __attribute__((aligned(16))) float s_hred[8 * 128];
    __shared__ __attribute__((aligned(16))) unsigned s_q[256];
    __shared__ __attribute__((aligned(16))) u16 s_xn[2 * 512];
    __shared__ __attribute__((aligned(16))) unsigned s_h[16];
    __shared__ __attribute__((aligned(16))) unsigned s_o[32];
    __shared__ __attribute__((aligned(16))) float s_ml[8 * 4 * 2];
    __shared__ __attribute__((aligned(16))) float s_fw[4 * 8];
    __shared__ __attribute__((aligned(16))) float s_fin[8 * 32];
    __shared__ unsigned s_flag, s_cnt;

    asm volatile("" :: "s"(x), "s"(Kc), "s"(Vc), "s"(wq), "s"(wo), "s"(w1), "s"(b1), "s"(w2), "s"(b2), "s"(out), "s"(ws));
    const int L = blockIdx.x;
    const int b = L >> 7, kvh = (L >> 4) & 7, s = L & 15, grp = L >> 4, c2 = L & 127;
    const int qline = (s + kvh) & 7;
    const int mi = 2 * b + (s >> 3);
    const int lane = threadIdx.x & 63, w0 = __builtin_amdgcn_readfirstlane(threadIdx.x >> 6);
    const int w = w0 - 1, tid = w * 64 + lane;
    const int rho = lane >> 4, j2 = lane & 7;
    const unsigned r16 = lane & 15;
    const __amdgpu_buffer_rsrc_t rs = __builtin_amdgcn_make_buffer_rsrc(ws, 0, WS_BYTES, 0x00020000);
    const v4u srd = {(unsigned)(uintptr_t)ws, (unsigned)((uintptr_t)ws >> 32) & 0xffffu, (unsigned)WS_BYTES, 0x00020000u};
    const bool m1_on = L < 224, m2_on = c2 < 112;
    const int mj = m1_on ? L % 28 : L - 224, mks = m1_on ? L / 28 : 0;
    const int hblk = m2_on ? c2 : c2 - 16;
    const bool fin_on = (s & 7) == kvh;
    const int fj = 2 * kvh + (s >> 3);

    if (w0 == 0) {
        __builtin_amdgcn_s_setprio(3);
        const v4u xa0 = *(const v4u*)(x + 16 * lane), xa1 = *(const v4u*)(x + 16 * lane + 8);
        CFENCE();
        const u64 epoch = __hip_atomic_load((GAS u64*)(ws + WS_EPOCH), RLX_AGENT);
        if (lane == 0) { s_flag = 0; s_cnt = 0; }
        BAR();
        const unsigned b1v = *(const unsigned*)(b1 + hblk * 16 + 2 * j2);
        const int ocol = 32 * fj + (lane & 31);
        const unsigned xo = x[b * 512 + ocol], b2o = b2[ocol];
        CFENCE();
        {
            const v4u sv = {SENT, SENT, SENT, SENT};
            const int base = WS_BLK + L * BLK_SZ + lane * 16, base2 = WS_IN2 + L * 2048 + lane * 16;
            ST_SC1(sv, base);
            ST_SC1(sv, base + 1024);
            ST_SC1(sv, lane < 8 ? base + 2048 : 0x7ffffff0);
            ST_SC1(sv, base2);
            ST_SC1(sv, base2 + 1024);
        }
        {
            float s1 = 0.f, s2 = 0.f, t1 = 0.f, t2 = 0.f;
#pragma unroll
            for (int k = 0; k < 4; ++k) {
                s1 = dot2(xa0[k], 0x3f803f80u, s1); t1 = dot2(xa1[k], 0x3f803f80u, t1);
                s2 = dot2(xa0[k], xa0[k], s2); t2 = dot2(xa1[k], xa1[k], t2);
            }
            s1 = row_sum(s1 + t1); s2 = row_sum(s2 + t2);
            s1 = sw16_add(s1, s1); s2 = sw16_add(s2, s2);
            const float mean = s1 * (1.0f / 512.0f);
            const float var = fmaxf(fmaf(-mean, mean, s2 * (1.0f / 512.0f)), 0.f);
            const float rstd = __builtin_amdgcn_rsqf(var + 1e-5f), nb = -mean * rstd;
            v4u o0, o1;
#pragma unroll
            for (int k = 0; k < 4; ++k) {
                o0[k] = pk_bf16(fmaf(bflo(xa0[k]), rstd, nb), fmaf(bfhi(xa0[k]), rstd, nb));
                o1[k] = pk_bf16(fmaf(bflo(xa1[k]), rstd, nb), fmaf(bfhi(xa1[k]), rstd, nb));
            }
            *(v4u*)&s_xn[16 * lane] = o0;
            *(v4u*)&s_xn[16 * lane + 8] = o1;
        }
        BAR();
        const u64 tag = epoch + 1ull;
        const unsigned tg0 = (unsigned)tag, tg1 = (unsigned)(tag >> 32), thi8 = (unsigned)(tag >> 8), tlo8 = tg0 << 24;
        asm volatile("" :: "v"(thi8), "v"(tlo8));
        BAR();
        {
            const int c = lane & 31, pos = (lane >> 5) * 64 + ((c >> 1) & 1) * 32 + (c & 1) * 16 + (c >> 2);
            float q0 = 0.f, q1 = 0.f, h0 = 0.f, h1 = 0.f;
#pragma unroll
            for (int k = 0; k < 8; ++k) { q0 += s_qred[k * 128 + pos]; q1 += s_qred[k * 128 + pos + 8]; }
            ST_SC1((v4u{tlo8 | (__float_as_uint(q0) >> 8), thi8, tlo8 | (__float_as_uint(q1) >> 8), thi8}),
                   WS_QA + (kvh * 8 + qline) * 4096 + rho * 1024 + mi * 256 + r16 * 16);
            CFENCE();
#pragma unroll
            for (int k = 0; k < 8; ++k) { h0 += s_hred[k * 128 + pos]; h1 += s_hred[k * 128 + pos + 8]; }
            if (m1_on)
                ST_SC1((v4u{tlo8 | (__float_as_uint(h0) >> 8), thi8, tlo8 | (__float_as_uint(h1) >> 8), thi8}),
                       WS_HG + ((lane >> 5) * 128 + 4 * mj + (c >> 3)) * 1024 + mks * 128 + (c & 7) * 16);
        }
        __builtin_amdgcn_s_sleep(24);
        {
            v4u ga;
            for (unsigned spins = 0;; ++spins) {
                CFENCE();
                ga = LD_SC1(WS_QA + (kvh * 8 + qline) * 4096 + mi * 1024 + lane * 16);
                const unsigned bad = (ga.y ^ thi8) | (ga.w ^ thi8) | (((ga.x ^ tlo8) | (ga.z ^ tlo8)) >> 24);
                if (__all(bad == 0u) || spins > SPIN_CAP) break;
                __builtin_amdgcn_s_sleep(4);
            }
            asm volatile("s_waitcnt vmcnt(0)" ::: "memory");
            float qa = __uint_as_float(ga.x << 8), qb = __uint_as_float(ga.z << 8);
            qa = sw16_add(qa, qa); qb = sw16_add(qb, qb);
            qa = sw32_add(qa, qa); qb = sw32_add(qb, qb);
            const unsigned pk = pk_bf16(qa, qb), pk2 = __float_as_uint(dpp<XOR1>(__uint_as_float(pk)));
            if (lane < 16 && !(lane & 1)) ST_SC1((v4u{pk, tg0, pk2, tg0}), WS_QG + grp * 2048 + (2 * qline + (s >> 3)) * 128 + lane * 8);
            if (lane == 16) __hip_atomic_store((GAS u64*)(ws + WS_FLAGS) + L, tag, RLX_AGENT);
        }
        __builtin_amdgcn_s_sleep(32);
        {
            v4u g[2];
            unsigned bad[2] = {1u, 1u};
            for (unsigned spins = 0;; ++spins) {
                CFENCE();
#pragma unroll
                for (int k = 0; k < 2; ++k) if (bad[k]) g[k] = LD_SC1(WS_QG + grp * 2048 + k * 1024 + lane * 16);
#pragma unroll
                for (int k = 0; k < 2; ++k) bad[k] = (g[k].y ^ tg0) | (g[k].w ^ tg0);
                if (__all((bad[0] | bad[1]) == 0u) || spins > SPIN_CAP) break;
                __builtin_amdgcn_s_sleep(4);
            }
            v4u gh = LD_SC1(WS_HG + (b * 128 + hblk) * 1024 + lane * 16);
            v4u gf = LD_SC1(WS_FLAGS + b * 1024 + lane * 16);
            CFENCE();
            *(v2u*)&s_q[2 * lane] = v2u{g[0].x, g[0].z};
            *(v2u*)&s_q[128 + 2 * lane] = v2u{g[1].x, g[1].z};
            LGKM0();
            if (lane == 0) __hip_atomic_store(&s_flag, 1u, RLX_WG);
            for (unsigned spins = 0;; ++spins) {
                const unsigned badh = (gh.y ^ thi8) | (gh.w ^ thi8) | (((gh.x ^ tlo8) | (gh.z ^ tlo8)) >> 24);
                const unsigned badf = (gf.x ^ tg0) | (gf.y ^ tg1) | (gf.z ^ tg0) | (gf.w ^ tg1);
                if (__all((badh | badf) == 0u) || spins > SPIN_CAP) break;
                __builtin_amdgcn_s_sleep(16);
                CFENCE();
                if (badh) gh = LD_SC1(WS_HG + (b * 128 + hblk) * 1024 + lane * 16);
                if (badf) gf = LD_SC1(WS_FLAGS + b * 1024 + lane * 16);
            }
            float hv[2] = {__uint_as_float(gh.x << 8), __uint_as_float(gh.z << 8)};
#pragma unroll
            for (int k = 0; k < 2; ++k) {
                float h = hv[k];
                h += dpp<ROR8>(h); h = sw16_add(h, h); h = sw32_add(h, h);
                h += k ? bfhi(b1v) : bflo(b1v);
                const float u2 = 1.5957691216057308f * (h + 0.044715f * h * h * h);
                const float th = 1.0f - 2.0f / (__expf(u2) + 1.0f);
                hv[k] = m2_on ? 0.5f * h * (1.0f + th) : 0.f;
            }
            if (lane < 8) *(v2u*)&s_h[2 * lane] = v2u{pk_bf16(hv[0], hv[0]), pk_bf16(hv[1], hv[1])};
        }
        LDS_WAIT(s_cnt, 8u, 2);
        CFENCE();
        {
            const int h = (lane >> 3) & 3;
            const f32x2 ml = *(const f32x2*)&s_ml[(j2 * 4 + h) * 2];
            const float M = oct_max(ml.x);
            const float f = __builtin_amdgcn_exp2f(ml.x - M);
            const float Ls = oct_sum(f * ml.y);
            if (lane < 32) s_fw[lane] = f;
            if (lane < 32 && j2 < 4) {
                const v2u mlv = {min(__float_as_uint(M), SENT - 1u), min(__float_as_uint(Ls), SENT - 1u)};
                __builtin_amdgcn_raw_buffer_store_b64(mlv, rs, WS_BLK + (grp * 16 + 4 * h + j2) * BLK_SZ + BLK_ML + s * 8, 0, AUX_SC1);
            }
        }
        BAR();
        v4u k0, k1, k2, k3;
        {
            const int base = WS_BLK + L * BLK_SZ;
            const int oa = base + BLK_IN1 + (2 * j2) * 128 + (lane >> 3) * 16;
            v4u ML;
            for (unsigned spins = 0;; ++spins) {
                CFENCE();
                ML = LD_SC1(base + BLK_ML + j2 * 16);
                if (__all(max4(ML) != SENT) || spins > SPIN_CAP) break;
                __builtin_amdgcn_s_sleep(2);
            }
            const float M0 = __uint_as_float(ML.x), L0 = __uint_as_float(ML.y), M1 = __uint_as_float(ML.z), L1 = __uint_as_float(ML.w);
            const float Mx = oct_max(fmaxf(M0, M1));
            float e0 = __builtin_amdgcn_exp2f(M0 - Mx), e1 = __builtin_amdgcn_exp2f(M1 - Mx);
            const float inv = __builtin_amdgcn_rcpf(oct_sum(fmaf(e0, L0, e1 * L1)));
            e0 *= inv; e1 *= inv;
            float o4[4];
            POLL_PAIR(128, o4, k0, k1, k2, k3, oa, srd, e0, e1);
            v4u ob;
#pragma unroll
            for (int t = 0; t < 4; ++t) { const float o = oct_sum(o4[t]); ob[t] = pk_bf16(o, o); }
            if (j2 == 0) *(v4u*)&s_o[(lane >> 3) * 4] = ob;
        }
        BAR();
        POLL_DRAIN(k0, k1, k2, k3);
        if (fin_on) {
            BAR();
            float r = bflo(xo) + bflo(b2o);
#pragma unroll
            for (int k = 0; k < 8; ++k) r += s_fin[k * 32 + (lane & 31)];
            if (lane < 32) out[b * 512 + ocol] = (u16)(pk_bf16(r, r) & 0xffffu);
            if (L == 0 && lane == 0) __hip_atomic_store((GAS u64*)(ws + WS_EPOCH), tag, RLX_AGENT);
        }
        return;
    }

    v4u wqr[2], w1r;
    {
        const u16* p = wq + (size_t)(128 * mi + 16 * w) * 4096 + kvh * 512 + qline * 64;
        const unsigned lo = (unsigned)(lane >> 3) * 4096u + 8u * j2;
        wqr[0] = *(const v4u*)(p + lo);
        wqr[1] = *(const v4u*)(p + lo + 8 * 4096);
        CFENCE();
        w1r = *(const v4u*)(w1 + (size_t)(64 * mks + 8 * w) * 1792 + mj * 64 + ((unsigned)(lane >> 3) * 1792u + 8u * j2));
    }
    CFENCE(); SB();
    BAR();
    const unsigned char* kb = (const unsigned char*)Kc + (size_t)grp * 8192 * 256;
    const unsigned char* vb = (const unsigned char*)Vc + (size_t)grp * 8192 * 256;
    const unsigned klo = (4u * (r16 & 3) + ((r16 >> 2) & 1)) * 256u + 64u * (r16 >> 3) + 16u * rho;
    const unsigned vlo = 16u * lane;
#define KIN(i) ((((i) >> 1) & 1) * 512 + ((i) & 1) * 128)
#define TOFF(u) ((unsigned)(((u) * 128 + s * 8 + w + 32 * grp) & 511) * 4096u)
    v4u kr[16], vr[16];
#pragma unroll
    for (int i = 0; i < K0; ++i) kr[i] = ld_nt(kb + TOFF(i >> 2) + klo + KIN(i));
    CFENCE(); SB();
    const f32x4 z4 = {0.f, 0.f, 0.f, 0.f};
    BAR();
    {
        const int xi = ((lane >> 2) & 1) * 512 + 2 * rho + ((lane >> 3) & 1);
        const unsigned sh = (lane & 1) * 16;
        const unsigned e0 = (unsigned)s_xn[xi + 128 * mi + 16 * w] << sh, e1 = (unsigned)s_xn[xi + 128 * mi + 16 * w + 8] << sh;
        const unsigned e2 = (unsigned)s_xn[xi + 64 * mks + 8 * w] << sh;
        const bool lo2 = (lane & 2) == 0;
        const unsigned a0 = lo2 ? e0 : 0u, c0 = lo2 ? 0u : e0, a1 = lo2 ? e1 : 0u, c1 = lo2 ? 0u : e1, a2 = lo2 ? e2 : 0u, c2_ = lo2 ? 0u : e2;
        const f32x4 d0 = mfma16(wqr[1].x, wqr[1].y, a1, c1, mfma16(wqr[0].x, wqr[0].y, a0, c0, z4));
        const f32x4 d1 = mfma16(wqr[1].z, wqr[1].w, a1, c1, mfma16(wqr[0].z, wqr[0].w, a0, c0, z4));
        f32x4 u;
#pragma unroll
        for (int i = 0; i < 4; ++i) {
            auto r = __builtin_amdgcn_permlane32_swap(__float_as_uint(d0[i]), __float_as_uint(d1[i]), false, false);
            u[i] = __uint_as_float(r[0]) + dpp<ROR8>(__uint_as_float(r[1]));
        }
        const int pos = w * 128 + ((lane >> 2) & 1) * 64 + (rho >> 1) * 32 + (lane & 3) * 8 + (rho & 1) * 4;
        if ((lane & 8) == 0) *(f32x4*)&s_qred[pos] = u;
        const f32x4 g0 = mfma16(w1r.x, w1r.y, a2, c2_, z4), g1 = mfma16(w1r.z, w1r.w, a2, c2_, z4);
#pragma unroll
        for (int i = 0; i < 4; ++i) {
            auto r = __builtin_amdgcn_permlane32_swap(__float_as_uint(g0[i]), __float_as_uint(g1[i]), false, false);
            u[i] = __uint_as_float(r[0]) + dpp<ROR8>(__uint_as_float(r[1]));
        }
        if ((lane & 8) == 0) *(f32x4*)&s_hred[pos] = u;
    }
    BAR();
    unsigned qrdy = 0;
#define WAITW() do { if (!qrdy) { qrdy = __builtin_amdgcn_readfirstlane(__hip_atomic_load(&s_flag, RLX_WG)); \
        if (!qrdy) asm volatile("s_waitcnt vmcnt(2)" ::: "memory"); } } while (0)
#pragma unroll
    for (int i = K0; i < 16; ++i) { WAITW(); kr[i] = ld_nt(kb + TOFF(i >> 2) + klo + KIN(i)); CFENCE(); }
#pragma unroll
    for (int i = 0; i < 16; ++i) { WAITW(); vr[i] = ld_nt(vb + TOFF(i >> 2) + vlo + (i & 3) * 1024); CFENCE(); }
    if (!qrdy) LDS_WAIT(s_flag, 1u, 1);
    CFENCE(); SB();
    constexpr float C2 = 0.08838834764831845f * 1.4426950408889634f;
    unsigned pp[4][4];
    {
        f32x4 S[4];
        v4u qf[2];
#pragma unroll
        for (int hf = 0; hf < 2; ++hf) qf[hf] = *(const v4u*)&s_q[(r16 >> 2) * 64 + 32 * hf + 16 * ((r16 >> 1) & 1) + 4 * rho];
#pragma unroll
        for (int u = 0; u < 4; ++u) {
            const f32x4 da = mfma32(kr[u * 4 + 1], qf[1], mfma32(kr[u * 4 + 0], qf[0], z4));
            const f32x4 db = mfma32(kr[u * 4 + 3], qf[1], mfma32(kr[u * 4 + 2], qf[0], z4));
#pragma unroll
            for (int i = 0; i < 4; ++i) {
                auto r = __builtin_amdgcn_permlane32_swap(__float_as_uint(da[i]), __float_as_uint(db[i]), false, false);
                S[u][i] = dpp<BC0>(__uint_as_float(r[0])) + dpp<BC2>(__uint_as_float(r[1]));
            }
        }
        float m = S[0][0];
#pragma unroll
        for (int u = 0; u < 4; ++u)
#pragma unroll
            for (int i = 0; i < 4; ++i) m = fmaxf(m, S[u][i]);
        m = sw16_max(m); m = sw32_max(m);
        m *= C2;
        float l4[4] = {0.f, 0.f, 0.f, 0.f};
#pragma unroll
        for (int u = 0; u < 4; ++u)
#pragma unroll
            for (int i = 0; i < 4; ++i) { const float p = __builtin_amdgcn_exp2f(fmaf(S[u][i], C2, -m)); l4[i] += p; pp[u][i] = pk_bf16(p, p); }
        float l = (l4[0] + l4[1]) + (l4[2] + l4[3]);
        l = sw16_add(l, l); l = sw32_add(l, l);
        if (lane < 16 && (lane & 3) == 0) *(f32x2*)&s_ml[(w * 4 + (lane >> 2)) * 2] = f32x2{m, l};
    }
    LGKM0();
    if (lane == 0) __hip_atomic_fetch_add(&s_cnt, 1u, RLX_WG);
    {
        const unsigned hm = (lane & 1) ? 0xffff0000u : 0x0000ffffu;
        const unsigned ma = (lane & 2) ? 0u : hm, mb = (lane & 2) ? hm : 0u;
        f32x4 O0 = z4, O1 = z4;
#pragma unroll
        for (int u = 0; u < 4; ++u)
#pragma unroll
            for (int i = 0; i < 4; ++i) {
                const unsigned pa = pp[u][i] & ma, pb = pp[u][i] & mb;
                const v4u v = vr[u * 4 + i];
                O0 = mfma16(v.x, v.y, pa, pb, O0);
                O1 = mfma16(v.z, v.w, pa, pb, O1);
            }
        float* po = &s_ored[(w * 128 + (r16 >> 2) * 32 + rho * 8 + (lane & 3)) * 4];
        *(f32x4*)po = O0;
        *(f32x4*)(po + 16) = O1;
    }
    int o_pub1 = WS_BLK + (grp * 16 + (tid >> 3)) * BLK_SZ + BLK_IN1 + s * 128 + j2 * 16;
    int o_pub2 = WS_IN2 + (b * 16 + 2 * w + rho) * 16384 + c2 * 128 + j2 * 16;
    int o_fin = WS_IN2 + (b * 16 + fj) * 16384 + w * 2048 + lane * 16;
    asm volatile("" : "+v"(o_pub1), "+v"(o_pub2), "+v"(o_fin));
    CFENCE(); SB();
    BAR();
    if (w < 2) {
        const f32x4 fa = *(const f32x4*)&s_fw[(tid >> 5) * 8], fb = *(const f32x4*)&s_fw[(tid >> 5) * 8 + 4];
        f32x4 acc = fa[0] * *(const f32x4*)&s_ored[tid * 4];
#pragma unroll
        for (int k = 1; k < 8; ++k) acc += (k < 4 ? fa[k & 3] : fb[k & 3]) * *(const f32x4*)&s_ored[(k * 128 + tid) * 4];
        ST_SC1(nosent(acc), o_pub1);
    }
    CFENCE();
    v4u wor[4], w2r[2];
    {
        const u16* p = wo + (size_t)(kvh * 512 + s * 32) * 512 + 64 * w;
        const u16* p2 = w2 + (size_t)hblk * 16 * 512 + 64 * w;
        const unsigned lo = (unsigned)(lane >> 3) * 512u + 8u * j2;
        w2r[0] = *(const v4u*)(p2 + lo); w2r[1] = *(const v4u*)(p2 + lo + 8 * 512);
#pragma unroll
        for (int t = 0; t < 4; ++t) wor[t] = *(const v4u*)(p + lo + t * 8 * 512);
    }
    CFENCE(); SB();
    unsigned msk[4];
#pragma unroll
    for (int k = 0; k < 4; ++k) msk[k] = (k == (j2 >> 1)) ? ((j2 & 1) ? 0xffff0000u : 0x0000ffffu) : 0u;
    const unsigned ph0 = s_h[lane >> 3], ph1 = s_h[(lane >> 3) + 8];
    v4u bh0 = {ph0 & msk[0], ph0 & msk[1], ph0 & msk[2], ph0 & msk[3]}, bh1 = {ph1 & msk[0], ph1 & msk[1], ph1 & msk[2], ph1 & msk[3]};
    asm volatile("" : "+v"(bh0), "+v"(bh1));
    BAR();
    {
        const v4u ob = *(const v4u*)&s_o[(lane >> 3) * 4];
        v4u bo[4];
#pragma unroll
        for (int t = 0; t < 4; ++t) bo[t] = v4u{ob[t] & msk[0], ob[t] & msk[1], ob[t] & msk[2], ob[t] & msk[3]};
        asm volatile("" : "+v"(bo[0]), "+v"(bo[1]), "+v"(bo[2]), "+v"(bo[3]));
        f32x4 Y = mfma32(w2r[1], bh1, mfma32(w2r[0], bh0, z4));
#pragma unroll
        for (int t = 0; t < 4; ++t) Y = mfma32(wor[t], bo[t], Y);
        f32x4 Z;
#pragma unroll
        for (int i = 0; i < 4; ++i) {
            const float y = dpp<ROR8>(Y[i]);
            auto r = __builtin_amdgcn_permlane32_swap(__float_as_uint(y), __float_as_uint(y), false, false);
            Z[i] = Y[i] + __uint_as_float(r[1]);
        }
        if (lane < 32 && (lane & 8) == 0) ST_SC1(nosent(Z), o_pub2);
    }
    if (fin_on) {
        v4u k0, k1, k2, k3;
        float v[4];
        const float one = 1.0f;
        POLL_PAIR(1024, v, k0, k1, k2, k3, o_fin, srd, one, one);
        const float t0 = sw32_add(v[0], v[1]), t1 = sw32_add(v[2], v[3]);
        float r = sw16_add(t0, t1);
        r += dpp<ROR8>(r);
        if ((lane & 8) == 0) s_fin[w * 32 + 8 * (2 * (rho & 1) + (rho >> 1)) + j2] = r;
        BAR();
        POLL_DRAIN(k0, k1, k2, k3);
    }
}

extern "C" void kernel_launch(void* const* d_in, const int* in_sizes, int n_in, void* d_out, int out_size, void* d_ws, size_t ws_size,
                              hipStream_t stream) {
    fused_decode<<<256, 576, 0, stream>>>((const u16*)d_in[0], (const u16*)d_in[1], (const u16*)d_in[2], (const u16*)d_in[3], (const u16*)d_in[4],
                                          (const u16*)d_in[6], (const u16*)d_in[7], (const u16*)d_in[8], (const u16*)d_in[9], (u16*)d_out,
                                          (unsigned char*)d_ws);
}
```

```cpp
#include <hip/hip_runtime.h>
#include <stdint.h>

typedef unsigned short u16;
typedef unsigned long long u64;
typedef unsigned v4u __attribute__((ext_vector_type(4)));
typedef unsigned v2u __attribute__((ext_vector_type(2)));
typedef float f32x4 __attribute__((ext_vector_type(4)));
typedef float f32x2 __attribute__((ext_vector_type(2)));
typedef short s16x4 __attribute__((ext_vector_type(4)));
typedef __bf16 bf16x8 __attribute__((ext_vector_type(8)));
typedef __bf16 bf16x2 __attribute__((ext_vector_type(2)));
#define GAS __attribute__((address_space(1)))
#define RLX_AGENT __ATOMIC_RELAXED, __HIP_MEMORY_SCOPE_AGENT
#define RLX_WG __ATOMIC_RELAXED, __HIP_MEMORY_SCOPE_WORKGROUP

#define K0V 5
#define RETRY 4
constexpr int K0 = K0V;
constexpr int WS_EPOCH = 0;
constexpr int WS_FLAGS = 4096;
constexpr int WS_QG = 8192;
constexpr int WS_BLK = 131072;
constexpr int BLK_SZ = 3072, BLK_IN1 = 0, BLK_ML = 2048;
constexpr int WS_IN2 = WS_BLK + 256 * BLK_SZ;
constexpr int WS_QA = WS_IN2 + 32 * 16384;
constexpr int WS_HG = WS_QA + 64 * 4096;
constexpr int WS_BYTES = WS_HG + 2 * 128 * 1024;
constexpr unsigned SENT = 0xFFFFFFFFu;
constexpr int AUX_SC1 = 16;
constexpr unsigned SPIN_CAP = 1u << 18;

template <int CTRL> __device__ __forceinline__ float dpp(float x) {
    return __builtin_bit_cast(float, __builtin_amdgcn_mov_dpp(__builtin_bit_cast(int, x), CTRL, 0xf, 0xf, true));
}
constexpr int XOR1 = 0xB1, XOR2 = 0x4E, HMIR = 0x141, ROR8 = 0x128, BC0 = 0x00, BC2 = 0xAA;
__device__ __forceinline__ float sw32_add(float a, float b) {
    auto r = __builtin_amdgcn_permlane32_swap(__float_as_uint(a), __float_as_uint(b), false, false);
    return __uint_as_float(r[0]) + __uint_as_float(r[1]);
}
__device__ __forceinline__ float sw16_add(float a, float b) {
    auto r = __builtin_amdgcn_permlane16_swap(__float_as_uint(a), __float_as_uint(b), false, false);
    return __uint_as_float(r[0]) + __uint_as_float(r[1]);
}
__device__ __forceinline__ float sw32_max(float a) {
    auto r = __builtin_amdgcn_permlane32_swap(__float_as_uint(a), __float_as_uint(a), false, false);
    return fmaxf(__uint_as_float(r[0]), __uint_as_float(r[1]));
}
__device__ __forceinline__ float sw16_max(float a) {
    auto r = __builtin_amdgcn_permlane16_swap(__float_as_uint(a), __float_as_uint(a), false, false);
    return fmaxf(__uint_as_float(r[0]), __uint_as_float(r[1]));
}
__device__ __forceinline__ float oct_sum(float x) { x += dpp<XOR1>(x); x += dpp<XOR2>(x); x += dpp<HMIR>(x); return x; }
__device__ __forceinline__ float oct_max(float x) { x = fmaxf(x, dpp<XOR1>(x)); x = fmaxf(x, dpp<XOR2>(x)); x = fmaxf(x, dpp<HMIR>(x)); return x; }
__device__ __forceinline__ float row_sum(float x) { x = oct_sum(x); x += dpp<ROR8>(x); return x; }
__device__ __forceinline__ float bflo(unsigned u) { return __uint_as_float(u << 16); }
__device__ __forceinline__ float bfhi(unsigned u) { return __uint_as_float(u & 0xffff0000u); }
__device__ __forceinline__ unsigned pk_bf16(float lo, float hi) {
    f32x2 v = {lo, hi};
    return __builtin_bit_cast(unsigned, __builtin_convertvector(v, bf16x2));
}
__device__ __forceinline__ float dot2(unsigned a, unsigned b, float c) {
    return __builtin_amdgcn_fdot2_f32_bf16(__builtin_bit_cast(bf16x2, a), __builtin_bit_cast(bf16x2, b), c, false);
}
__device__ __forceinline__ f32x4 mfma32(v4u a, v4u b, f32x4 c) {
    return __builtin_amdgcn_mfma_f32_16x16x32_bf16(__builtin_bit_cast(bf16x8, a), __builtin_bit_cast(bf16x8, b), c, 0, 0, 0);
}
__device__ __forceinline__ f32x4 mfma16(unsigned a0, unsigned a1, unsigned b0, unsigned b1, f32x4 c) {
    return __builtin_amdgcn_mfma_f32_16x16x16bf16_1k(__builtin_bit_cast(s16x4, v2u{a0, a1}), __builtin_bit_cast(s16x4, v2u{b0, b1}), c, 0, 0, 0);
}
__device__ __forceinline__ v4u ld_nt(const unsigned char* p) { return __builtin_nontemporal_load((const v4u*)p); }
__device__ __forceinline__ v4u nosent(f32x4 v) {
    v4u u = __builtin_bit_cast(v4u, v);
    u.x = min(u.x, SENT - 1u); u.y = min(u.y, SENT - 1u); u.z = min(u.z, SENT - 1u); u.w = min(u.w, SENT - 1u);
    return u;
}
__device__ __forceinline__ unsigned max4(v4u a) { return max(max(a.x, a.y), max(a.z, a.w)); }

#define POLL_CHECK(b0, b1, b2, b3, b4, b5, b6, b7) \
    "v_max3_u32 v144, v" #b0 ", v" #b1 ", v" #b2 "\n\tv_max3_u32 v145, v" #b3 ", v" #b4 ", v" #b5 "\n\t" \
    "v_max3_u32 v144, v144, v" #b6 ", v" #b7 "\n\tv_max_u32 v144, v144, v145\n\tv_cmp_eq_u32 vcc, -1, v144\n\t" \
    "v_mul_f32 %0, %11, v" #b0 "\n\tv_mul_f32 %1, %11, v" #b1 "\n\tv_mul_f32 %2, %11, v" #b2 "\n\tv_mul_f32 %3, %11, v" #b3 "\n\t" \
    "v_fmac_f32 %0, %12, v" #b4 "\n\tv_fmac_f32 %1, %12, v" #b5 "\n\tv_fmac_f32 %2, %12, v" #b6 "\n\tv_fmac_f32 %3, %12, v" #b7 "\n\t" \
    "s_mov_b64 exec, vcc\n\t" \
    "s_cbranch_vccz 2f\n\t"
#define POLL_PAIR(D, r, k0, k1, k2, k3, off, srd, e0, e1) do { unsigned cnt_; u64 ex_; \
    asm volatile( \
        "s_mov_b64 %9, exec\n\t" \
        "buffer_load_dwordx4 v[128:131], %10, %13, 0 offen sc1\n\t" \
        "buffer_load_dwordx4 v[132:135], %10, %13, 0 offen offset:" #D " sc1\n\t" \
        "s_mov_b32 %4, 0x40000\n\t" \
        "s_sleep 8\n" \
        "1:\n\t" \
        "buffer_load_dwordx4 v[136:139], %10, %13, 0 offen sc1\n\t" \
        "buffer_load_dwordx4 v[140:143], %10, %13, 0 offen offset:" #D " sc1\n\t" \
        "s_waitcnt vmcnt(2)\n\t" \
        POLL_CHECK(128, 129, 130, 131, 132, 133, 134, 135) \
        "buffer_load_dwordx4 v[128:131], %10, %13, 0 offen sc1\n\t" \
        "buffer_load_dwordx4 v[132:135], %10, %13, 0 offen offset:" #D " sc1\n\t" \
        "s_waitcnt vmcnt(2)\n\t" \
        POLL_CHECK(136, 137, 138, 139, 140, 141, 142, 143) \
        "s_sub_u32 %4, %4, 1\n\t" \
        "s_cbranch_scc0 1b\n" \
        "2:\n\t" \
        "s_mov_b64 exec, %9\n\t" \
        "s_nop 4" \
        : "=&v"(r[0]), "=&v"(r[1]), "=&v"(r[2]), "=&v"(r[3]), "=&s"(cnt_), \
          "=&{v[128:131]}"(k0), "=&{v[132:135]}"(k1), "=&{v[136:139]}"(k2), "=&{v[140:143]}"(k3), "=&s"(ex_) \
        : "v"(off), "v"(e0), "v"(e1), "s"(srd) \
        : "v144", "v145", "vcc", "scc", "memory"); } while (0)
#define POLL_DRAIN(k0, k1, k2, k3) \
    asm volatile("s_waitcnt vmcnt(0)" :: "{v[128:131]}"(k0), "{v[132:135]}"(k1), "{v[136:139]}"(k2), "{v[140:143]}"(k3) : "memory")
#define SB() __builtin_amdgcn_sched_barrier(0)
#define CFENCE() asm volatile("" ::: "memory")
#define LGKM0() asm volatile("s_waitcnt lgkmcnt(0)" ::: "memory")
#define BAR() do { asm volatile("s_waitcnt lgkmcnt(0)" ::: "memory"); __builtin_amdgcn_s_barrier(); asm volatile("" ::: "memory"); } while (0)
#define LDS_WAIT(word, target, slp) \
    for (unsigned spins_ = 0; __hip_atomic_load(&(word), RLX_WG) < (target) && spins_ < (SPIN_CAP << 4); ++spins_) __builtin_amdgcn_s_sleep(slp)
#define LD_SC1(off) __builtin_amdgcn_raw_buffer_load_b128(rs, (off), 0, AUX_SC1)
#define ST_SC1(v, off) __builtin_amdgcn_raw_buffer_store_b128((v), rs, (off), 0, AUX_SC1)

__global__ void __launch_bounds__(576, 3)
fused_decode(const u16* __restrict__ x, const u16* __restrict__ Kc, const u16* __restrict__ Vc, const u16* __restrict__ wq,
             const u16* __restrict__ wo, const u16* __restrict__ w1, const u16* __restrict__ b1, const u16* __restrict__ w2,
             const u16* __restrict__ b2, u16* __restrict__ out, unsigned char* __restrict__ ws) {
    __shared__ __attribute__((aligned(16))) float s_ored[8 * 128 * 4];
    __shared__ __attribute__((aligned(16))) float s_qred[8 * 128];
    __shared__ __attribute__((aligned(16))) float s_hred[8 * 128];
    __shared__ __attribute__((aligned(16))) unsigned s_q[256];
    __shared__ __attribute__((aligned(16))) u16 s_xn[2 * 512];
    __shared__ __attribute__((aligned(16))) unsigned s_h[16];
    __shared__ __attribute__((aligned(16))) unsigned s_o[32];
    __shared__ __attribute__((aligned(16))) float s_ml[8 * 4 * 2];
    __shared__ __attribute__((aligned(16))) float s_fw[4 * 8];
    __shared__ __attribute__((aligned(16))) float s_fin[8 * 32];
    __shared__ unsigned s_flag, s_cnt;

    asm volatile("" :: "s"(x), "s"(Kc), "s"(Vc), "s"(wq), "s"(wo), "s"(w1), "s"(b1), "s"(w2), "s"(b2), "s"(out), "s"(ws));
    const int L = blockIdx.x;
    const int b = L >> 7, kvh = (L >> 4) & 7, s = L & 15, grp = L >> 4, c2 = L & 127;
    const int qline = (s + kvh) & 7;
    const int mi = 2 * b + (s >> 3);
    const int lane = threadIdx.x & 63, w0 = __builtin_amdgcn_readfirstlane(threadIdx.x >> 6);
    const int w = w0 - 1, tid = w * 64 + lane;
    const int rho = lane >> 4, j2 = lane & 7;
    const unsigned r16 = lane & 15;
    const __amdgpu_buffer_rsrc_t rs = __builtin_amdgcn_make_buffer_rsrc(ws, 0, WS_BYTES, 0x00020000);
    const v4u srd = {(unsigned)(uintptr_t)ws, (unsigned)((uintptr_t)ws >> 32) & 0xffffu, (unsigned)WS_BYTES, 0x00020000u};
    const bool m1_on = L < 224, m2_on = c2 < 112;
    const int mj = m1_on ? L % 28 : L - 224, mks = m1_on ? L / 28 : 0;
    const int hblk = m2_on ? c2 : c2 - 16;
    const bool fin_on = (s & 7) == kvh;
    const int fj = 2 * kvh + (s >> 3);

    if (w0 == 0) {
        __builtin_amdgcn_s_setprio(3);
        const v4u xa0 = *(const v4u*)(x + 16 * lane), xa1 = *(const v4u*)(x + 16 * lane + 8);
        CFENCE();
        const u64 epoch = __hip_atomic_load((GAS u64*)(ws + WS_EPOCH), RLX_AGENT);
        if (lane == 0) { s_flag = 0; s_cnt = 0; }
        BAR();
        const unsigned b1v = *(const unsigned*)(b1 + hblk * 16 + 2 * j2);
        const int ocol = 32 * fj + (lane & 31);
        const unsigned xo = x[b * 512 + ocol], b2o = b2[ocol];
        CFENCE();
        {
            const v4u sv = {SENT, SENT, SENT, SENT};
            const int base = WS_BLK + L * BLK_SZ + lane * 16, base2 = WS_IN2 + L * 2048 + lane * 16;
            ST_SC1(sv, base);
            ST_SC1(sv, base + 1024);
            ST_SC1(sv, lane < 8 ? base + 2048 : 0x7ffffff0);
            ST_SC1(sv, base2);
            ST_SC1(sv, base2 + 1024);
        }
        {
            float s1 = 0.f, s2 = 0.f, t1 = 0.f, t2 = 0.f;
#pragma unroll
            for (int k = 0; k < 4; ++k) {
                s1 = dot2(xa0[k], 0x3f803f80u, s1); t1 = dot2(xa1[k], 0x3f803f80u, t1);
                s2 = dot2(xa0[k], xa0[k], s2); t2 = dot2(xa1[k], xa1[k], t2);
            }
            s1 = row_sum(s1 + t1); s2 = row_sum(s2 + t2);
            s1 = sw16_add(s1, s1); s2 = sw16_add(s2, s2);
            const float mean = s1 * (1.0f / 512.0f);
            const float var = fmaxf(fmaf(-mean, mean, s2 * (1.0f / 512.0f)), 0.f);
            const float rstd = __builtin_amdgcn_rsqf(var + 1e-5f), nb = -mean * rstd;
            v4u o0, o1;
#pragma unroll
            for (int k = 0; k < 4; ++k) {
                o0[k] = pk_bf16(fmaf(bflo(xa0[k]), rstd, nb), fmaf(bfhi(xa0[k]), rstd, nb));
                o1[k] = pk_bf16(fmaf(bflo(xa1[k]), rstd, nb), fmaf(bfhi(xa1[k]), rstd, nb));
            }
            *(v4u*)&s_xn[16 * lane] = o0;
            *(v4u*)&s_xn[16 * lane + 8] = o1;
        }
        BAR();
        const u64 tag = epoch + 1ull;
        const unsigned tg0 = (unsigned)tag, tg1 = (unsigned)(tag >> 32), thi8 = (unsigned)(tag >> 8), tlo8 = tg0 << 24;
        asm volatile("" :: "v"(thi8), "v"(tlo8));
        BAR();
        {
            const int c = lane & 31, pos = (lane >> 5) * 64 + ((c >> 1) & 1) * 32 + (c & 1) * 16 + (c >> 2);
            float q0 = 0.f, q1 = 0.f, h0 = 0.f, h1 = 0.f;
#pragma unroll
            for (int k = 0; k < 8; ++k) { q0 += s_qred[k * 128 + pos]; q1 += s_qred[k * 128 + pos + 8]; }
            ST_SC1((v4u{tlo8 | (__float_as_uint(q0) >> 8), thi8, tlo8 | (__float_as_uint(q1) >> 8), thi8}),
                   WS_QA + (kvh * 8 + qline) * 4096 + rho * 1024 + mi * 256 + r16 * 16);
            CFENCE();
#pragma unroll
            for (int k = 0; k < 8; ++k) { h0 += s_hred[k * 128 + pos]; h1 += s_hred[k * 128 + pos + 8]; }
            if (m1_on)
                ST_SC1((v4u{tlo8 | (__float_as_uint(h0) >> 8), thi8, tlo8 | (__float_as_uint(h1) >> 8), thi8}),
                       WS_HG + ((lane >> 5) * 128 + 4 * mj + (c >> 3)) * 1024 + mks * 128 + (c & 7) * 16);
        }
        __builtin_amdgcn_s_sleep(24);
        {
            v4u ga;
            for (unsigned spins = 0;; ++spins) {
                CFENCE();
                ga = LD_SC1(WS_QA + (kvh * 8 + qline) * 4096 + mi * 1024 + lane * 16);
                const unsigned bad = (ga.y ^ thi8) | (ga.w ^ thi8) | (((ga.x ^ tlo8) | (ga.z ^ tlo8)) >> 24);
                if (__all(bad == 0u) || spins > SPIN_CAP) break;
                __builtin_amdgcn_s_sleep(4);
            }
            asm volatile("s_waitcnt vmcnt(0)" ::: "memory");
            float qa = __uint_as_float(ga.x << 8), qb = __uint_as_float(ga.z << 8);
            qa = sw16_add(qa, qa); qb = sw16_add(qb, qb);
            qa = sw32_add(qa, qa); qb = sw32_add(qb, qb);
            const unsigned pk = pk_bf16(qa, qb), pk2 = __float_as_uint(dpp<XOR1>(__uint_as_float(pk)));
            if (lane < 16 && !(lane & 1)) ST_SC1((v4u{pk, tg0, pk2, tg0}), WS_QG + grp * 2048 + (2 * qline + (s >> 3)) * 128 + lane * 8);
            if (lane == 16) __hip_atomic_store((GAS u64*)(ws + WS_FLAGS) + L, tag, RLX_AGENT);
        }
        __builtin_amdgcn_s_sleep(32);
        {
            v4u g[2], gh, gf;
            unsigned bad[2] = {1u, 1u}, badh = 1u, badf = 1u;
            bool qdone = false;
            for (unsigned spins = 0;; ++spins) {
                CFENCE();
#pragma unroll
                for (int k = 0; k < 2; ++k) if (bad[k]) g[k] = LD_SC1(WS_QG + grp * 2048 + k * 1024 + lane * 16);
                if (badh) gh = LD_SC1(WS_HG + (b * 128 + hblk) * 1024 + lane * 16);
                if (badf) gf = LD_SC1(WS_FLAGS + b * 1024 + lane * 16);
#pragma unroll
                for (int k = 0; k < 2; ++k) bad[k] = (g[k].y ^ tg0) | (g[k].w ^ tg0);
                if (!qdone && (__all((bad[0] | bad[1]) == 0u) || spins > SPIN_CAP)) {
                    qdone = true;
                    *(v2u*)&s_q[2 * lane] = v2u{g[0].x, g[0].z};
                    *(v2u*)&s_q[128 + 2 * lane] = v2u{g[1].x, g[1].z};
                    LGKM0();
                    if (lane == 0) __hip_atomic_store(&s_flag, 1u, RLX_WG);
                }
                badh = (gh.y ^ thi8) | (gh.w ^ thi8) | (((gh.x ^ tlo8) | (gh.z ^ tlo8)) >> 24);
                badf = (gf.x ^ tg0) | (gf.y ^ tg1) | (gf.z ^ tg0) | (gf.w ^ tg1);
                if ((qdone && __all((badh | badf) == 0u)) || spins > SPIN_CAP) break;
                __builtin_amdgcn_s_sleep(RETRY);
            }
            if (lane == 0) __hip_atomic_store(&s_flag, 2u, RLX_WG);
            float hv[2] = {__uint_as_float(gh.x << 8), __uint_as_float(gh.z << 8)};
#pragma unroll
            for (int k = 0; k < 2; ++k) {
                float h = hv[k];
                h += dpp<ROR8>(h); h = sw16_add(h, h); h = sw32_add(h, h);
                h += k ? bfhi(b1v) : bflo(b1v);
                const float u2 = 1.5957691216057308f * (h + 0.044715f * h * h * h);
                const float th = 1.0f - 2.0f / (__expf(u2) + 1.0f);
                hv[k] = m2_on ? 0.5f * h * (1.0f + th) : 0.f;
            }
            if (lane < 8) *(v2u*)&s_h[2 * lane] = v2u{pk_bf16(hv[0], hv[0]), pk_bf16(hv[1], hv[1])};
        }
        LDS_WAIT(s_cnt, 8u, 2);
        CFENCE();
        {
            const int h = (lane >> 3) & 3;
            const f32x2 ml = *(const f32x2*)&s_ml[(j2 * 4 + h) * 2];
            const float M = oct_max(ml.x);
            const float f = __builtin_amdgcn_exp2f(ml.x - M);
            const float Ls = oct_sum(f * ml.y);
            if (lane < 32) s_fw[lane] = f;
            if (lane < 32 && j2 < 4) {
                const v2u mlv = {min(__float_as_uint(M), SENT - 1u), min(__float_as_uint(Ls), SENT - 1u)};
                __builtin_amdgcn_raw_buffer_store_b64(mlv, rs, WS_BLK + (grp * 16 + 4 * h + j2) * BLK_SZ + BLK_ML + s * 8, 0, AUX_SC1);
            }
        }
        BAR();
        v4u k0, k1, k2, k3;
        {
            const int base = WS_BLK + L * BLK_SZ;
            const int oa = base + BLK_IN1 + (2 * j2) * 128 + (lane >> 3) * 16;
            v4u ML;
            for (unsigned spins = 0;; ++spins) {
                CFENCE();
                ML = LD_SC1(base + BLK_ML + j2 * 16);
                if (__all(max4(ML) != SENT) || spins > SPIN_CAP) break;
                __builtin_amdgcn_s_sleep(2);
            }
            const float M0 = __uint_as_float(ML.x), L0 = __uint_as_float(ML.y), M1 = __uint_as_float(ML.z), L1 = __uint_as_float(ML.w);
            const float Mx = oct_max(fmaxf(M0, M1));
            float e0 = __builtin_amdgcn_exp2f(M0 - Mx), e1 = __builtin_amdgcn_exp2f(M1 - Mx);
            const float inv = __builtin_amdgcn_rcpf(oct_sum(fmaf(e0, L0, e1 * L1)));
            e0 *= inv; e1 *= inv;
            float o4[4];
            POLL_PAIR(128, o4, k0, k1, k2, k3, oa, srd, e0, e1);
            v4u ob;
#pragma unroll
            for (int t = 0; t < 4; ++t) { const float o = oct_sum(o4[t]); ob[t] = pk_bf16(o, o); }
            if (j2 == 0) *(v4u*)&s_o[(lane >> 3) * 4] = ob;
        }
        BAR();
        POLL_DRAIN(k0, k1, k2, k3);
        if (fin_on) {
            BAR();
            float r = bflo(xo) + bflo(b2o);
#pragma unroll
            for (int k = 0; k < 8; ++k) r += s_fin[k * 32 + (lane & 31)];
            if (lane < 32) out[b * 512 + ocol] = (u16)(pk_bf16(r, r) & 0xffffu);
            if (L == 0 && lane == 0) __hip_atomic_store((GAS u64*)(ws + WS_EPOCH), tag, RLX_AGENT);
        }
        return;
    }

    v4u wqr[2], w1r;
    {
        const u16* p = wq + (size_t)(128 * mi + 16 * w) * 4096 + kvh * 512 + qline * 64;
        const unsigned lo = (unsigned)(lane >> 3) * 4096u + 8u * j2;
        wqr[0] = *(const v4u*)(p + lo);
        wqr[1] = *(const v4u*)(p + lo + 8 * 4096);
        CFENCE();
        w1r = *(const v4u*)(w1 + (size_t)(64 * mks + 8 * w) * 1792 + mj * 64 + ((unsigned)(lane >> 3) * 1792u + 8u * j2));
    }
    CFENCE(); SB();
    BAR();
    const unsigned char* kb = (const unsigned char*)Kc + (size_t)grp * 8192 * 256;
    const unsigned char* vb = (const unsigned char*)Vc + (size_t)grp * 8192 * 256;
    const unsigned klo = (4u * (r16 & 3) + ((r16 >> 2) & 1)) * 256u + 64u * (r16 >> 3) + 16u * rho;
    const unsigned vlo = 16u * lane;
#define KIN(i) ((((i) >> 1) & 1) * 512 + ((i) & 1) * 128)
#define TOFF(u) ((unsigned)(((u) * 128 + s * 8 + w + 32 * grp) & 511) * 4096u)
    v4u kr[16], vr[16];
#pragma unroll
    for (int i = 0; i < K0; ++i) kr[i] = ld_nt(kb + TOFF(i >> 2) + klo + KIN(i));
    CFENCE(); SB();
    const f32x4 z4 = {0.f, 0.f, 0.f, 0.f};
    BAR();
    {
        const int xi = ((lane >> 2) & 1) * 512 + 2 * rho + ((lane >> 3) & 1);
        const unsigned sh = (lane & 1) * 16;
        const unsigned e0 = (unsigned)s_xn[xi + 128 * mi + 16 * w] << sh, e1 = (unsigned)s_xn[xi + 128 * mi + 16 * w + 8] << sh;
        const unsigned e2 = (unsigned)s_xn[xi + 64 * mks + 8 * w] << sh;
        const bool lo2 = (lane & 2) == 0;
        const unsigned a0 = lo2 ? e0 : 0u, c0 = lo2 ? 0u : e0, a1 = lo2 ? e1 : 0u, c1 = lo2 ? 0u : e1, a2 = lo2 ? e2 : 0u, c2_ = lo2 ? 0u : e2;
        const f32x4 d0 = mfma16(wqr[1].x, wqr[1].y, a1, c1, mfma16(wqr[0].x, wqr[0].y, a0, c0, z4));
        const f32x4 d1 = mfma16(wqr[1].z, wqr[1].w, a1, c1, mfma16(wqr[0].z, wqr[0].w, a0, c0, z4));
        f32x4 u;
#pragma unroll
        for (int i = 0; i < 4; ++i) {
            auto r = __builtin_amdgcn_permlane32_swap(__float_as_uint(d0[i]), __float_as_uint(d1[i]), false, false);
            u[i] = __uint_as_float(r[0]) + dpp<ROR8>(__uint_as_float(r[1]));
        }
        const int pos = w * 128 + ((lane >> 2) & 1) * 64 + (rho >> 1) * 32 + (lane & 3) * 8 + (rho & 1) * 4;
        if ((lane & 8) == 0) *(f32x4*)&s_qred[pos] = u;
        const f32x4 g0 = mfma16(w1r.x, w1r.y, a2, c2_, z4), g1 = mfma16(w1r.z, w1r.w, a2, c2_, z4);
#pragma unroll
        for (int i = 0; i < 4; ++i) {
            auto r = __builtin_amdgcn_permlane32_swap(__float_as_uint(g0[i]), __float_as_uint(g1[i]), false, false);
            u[i] = __uint_as_float(r[0]) + dpp<ROR8>(__uint_as_float(r[1]));
        }
        if ((lane & 8) == 0) *(f32x4*)&s_hred[pos] = u;
    }
    BAR();
    unsigned qrdy = 0;
#define WAITW() do { if (!qrdy) { qrdy = __builtin_amdgcn_readfirstlane(__hip_atomic_load(&s_flag, RLX_WG)) >> 1; \
        if (!qrdy) asm volatile("s_waitcnt vmcnt(2)" ::: "memory"); } } while (0)
#pragma unroll
    for (int i = K0; i < 16; ++i) { WAITW(); kr[i] = ld_nt(kb + TOFF(i >> 2) + klo + KIN(i)); CFENCE(); }
#pragma unroll
    for (int i = 0; i < 16; ++i) { WAITW(); vr[i] = ld_nt(vb + TOFF(i >> 2) + vlo + (i & 3) * 1024); CFENCE(); }
    if (!qrdy) LDS_WAIT(s_flag, 1u, 1);
    CFENCE(); SB();
    constexpr float C2 = 0.08838834764831845f * 1.4426950408889634f;
    unsigned pp[4][4];
    {
        f32x4 S[4];
        v4u qf[2];
#pragma unroll
        for (int hf = 0; hf < 2; ++hf) qf[hf] = *(const v4u*)&s_q[(r16 >> 2) * 64 + 32 * hf + 16 * ((r16 >> 1) & 1) + 4 * rho];
#pragma unroll
        for (int u = 0; u < 4; ++u) {
            const f32x4 da = mfma32(kr[u * 4 + 1], qf[1], mfma32(kr[u * 4 + 0], qf[0], z4));
            const f32x4 db = mfma32(kr[u * 4 + 3], qf[1], mfma32(kr[u * 4 + 2], qf[0], z4));
#pragma unroll
            for (int i = 0; i < 4; ++i) {
                auto r = __builtin_amdgcn_permlane32_swap(__float_as_uint(da[i]), __float_as_uint(db[i]), false, false);
                S[u][i] = dpp<BC0>(__uint_as_float(r[0])) + dpp<BC2>(__uint_as_float(r[1]));
            }
        }
        float m = S[0][0];
#pragma unroll
        for (int u = 0; u < 4; ++u)
#pragma unroll
            for (int i = 0; i < 4; ++i) m = fmaxf(m, S[u][i]);
        m = sw16_max(m); m = sw32_max(m);
        m *= C2;
        float l4[4] = {0.f, 0.f, 0.f, 0.f};
#pragma unroll
        for (int u = 0; u < 4; ++u)
#pragma unroll
            for (int i = 0; i < 4; ++i) { const float p = __builtin_amdgcn_exp2f(fmaf(S[u][i], C2, -m)); l4[i] += p; pp[u][i] = pk_bf16(p, p); }
        float l = (l4[0] + l4[1]) + (l4[2] + l4[3]);
        l = sw16_add(l, l); l = sw32_add(l, l);
        if (lane < 16 && (lane & 3) == 0) *(f32x2*)&s_ml[(w * 4 + (lane >> 2)) * 2] = f32x2{m, l};
    }
    LGKM0();
    if (lane == 0) __hip_atomic_fetch_add(&s_cnt, 1u, RLX_WG);
    {
        const unsigned hm = (lane & 1) ? 0xffff0000u : 0x0000ffffu;
        const unsigned ma = (lane & 2) ? 0u : hm, mb = (lane & 2) ? hm : 0u;
        f32x4 O0 = z4, O1 = z4;
#pragma unroll
        for (int u = 0; u < 4; ++u)
#pragma unroll
            for (int i = 0; i < 4; ++i) {
                const unsigned pa = pp[u][i] & ma, pb = pp[u][i] & mb;
                const v4u v = vr[u * 4 + i];
                O0 = mfma16(v.x, v.y, pa, pb, O0);
                O1 = mfma16(v.z, v.w, pa, pb, O1);
            }
        float* po = &s_ored[(w * 128 + (r16 >> 2) * 32 + rho * 8 + (lane & 3)) * 4];
        *(f32x4*)po = O0;
        *(f32x4*)(po + 16) = O1;
    }
    int o_pub1 = WS_BLK + (grp * 16 + (tid >> 3)) * BLK_SZ + BLK_IN1 + s * 128 + j2 * 16;
    int o_pub2 = WS_IN2 + (b * 16 + 2 * w + rho) * 16384 + c2 * 128 + j2 * 16;
    int o_fin = WS_IN2 + (b * 16 + fj) * 16384 + w * 2048 + lane * 16;
    asm volatile("" : "+v"(o_pub1), "+v"(o_pub2), "+v"(o_fin));
    CFENCE(); SB();
    BAR();
    if (w < 2) {
        const f32x4 fa = *(const f32x4*)&s_fw[(tid >> 5) * 8], fb = *(const f32x4*)&s_fw[(tid >> 5) * 8 + 4];
        f32x4 acc = fa[0] * *(const f32x4*)&s_ored[tid * 4];
#pragma unroll
        for (int k = 1; k < 8; ++k) acc += (k < 4 ? fa[k & 3] : fb[k & 3]) * *(const f32x4*)&s_ored[(k * 128 + tid) * 4];
        ST_SC1(nosent(acc), o_pub1);
    }
    CFENCE();
    v4u wor[4], w2r[2];
    {
        const u16* p = wo + (size_t)(kvh * 512 + s * 32) * 512 + 64 * w;
        const u16* p2 = w2 + (size_t)hblk * 16 * 512 + 64 * w;
        const unsigned lo = (unsigned)(lane >> 3) * 512u + 8u * j2;
        w2r[0] = *(const v4u*)(p2 + lo); w2r[1] = *(const v4u*)(p2 + lo + 8 * 512);
#pragma unroll
        for (int t = 0; t < 4; ++t) wor[t] = *(const v4u*)(p + lo + t * 8 * 512);
    }
    CFENCE(); SB();
    unsigned msk[4];
#pragma unroll
    for (int k = 0; k < 4; ++k) msk[k] = (k == (j2 >> 1)) ? ((j2 & 1) ? 0xffff0000u : 0x0000ffffu) : 0u;
    const unsigned ph0 = s_h[lane >> 3], ph1 = s_h[(lane >> 3) + 8];
    v4u bh0 = {ph0 & msk[0], ph0 & msk[1], ph0 & msk[2], ph0 & msk[3]}, bh1 = {ph1 & msk[0], ph1 & msk[1], ph1 & msk[2], ph1 & msk[3]};
    asm volatile("" : "+v"(bh0), "+v"(bh1));
    BAR();
    {
        const v4u ob = *(const v4u*)&s_o[(lane >> 3) * 4];
        v4u bo[4];
#pragma unroll
        for (int t = 0; t < 4; ++t) bo[t] = v4u{ob[t] & msk[0], ob[t] & msk[1], ob[t] & msk[2], ob[t] & msk[3]};
        asm volatile("" : "+v"(bo[0]), "+v"(bo[1]), "+v"(bo[2]), "+v"(bo[3]));
        f32x4 Y = mfma32(w2r[1], bh1, mfma32(w2r[0], bh0, z4));
#pragma unroll
        for (int t = 0; t < 4; ++t) Y = mfma32(wor[t], bo[t], Y);
        f32x4 Z;
#pragma unroll
        for (int i = 0; i < 4; ++i) {
            const float y = dpp<ROR8>(Y[i]);
            auto r = __builtin_amdgcn_permlane32_swap(__float_as_uint(y), __float_as_uint(y), false, false);
            Z[i] = Y[i] + __uint_as_float(r[1]);
        }
        if (lane < 32 && (lane & 8) == 0) ST_SC1(nosent(Z), o_pub2);
    }
    if (fin_on) {
        v4u k0, k1, k2, k3;
        float v[4];
        const float one = 1.0f;
        POLL_PAIR(1024, v, k0, k1, k2, k3, o_fin, srd, one, one);
        const float t0 = sw32_add(v[0], v[1]), t1 = sw32_add(v[2], v[3]);
        float r = sw16_add(t0, t1);
        r += dpp<ROR8>(r);
        if ((lane & 8) == 0) s_fin[w * 32 + 8 * (2 * (rho & 1) + (rho >> 1)) + j2] = r;
        BAR();
        POLL_DRAIN(k0, k1, k2, k3);
    }
}

extern "C" void kernel_launch(void* const* d_in, const int* in_sizes, int n_in, void* d_out, int out_size, void* d_ws, size_t ws_size,
                              hipStream_t stream) {
    fused_decode<<<256, 576, 0, stream>>>((const u16*)d_in[0], (const u16*)d_in[1], (const u16*)d_in[2], (const u16*)d_in[3], (const u16*)d_in[4],
                                          (const u16*)d_in[6], (const u16*)d_in[7], (const u16*)d_in[8], (const u16*)d_in[9], (u16*)d_out,
                                          (unsigned char*)d_ws);
}
```

```cpp
#include <hip/hip_runtime.h>
#include <stdint.h>

typedef unsigned short u16;
typedef unsigned long long u64;
typedef unsigned v4u __attribute__((ext_vector_type(4)));
typedef unsigned v2u __attribute__((ext_vector_type(2)));
typedef float f32x4 __attribute__((ext_vector_type(4)));
typedef float f32x2 __attribute__((ext_vector_type(2)));
typedef short s16x4 __attribute__((ext_vector_type(4)));
typedef __bf16 bf16x8 __attribute__((ext_vector_type(8)));
typedef __bf16 bf16x2 __attribute__((ext_vector_type(2)));
#define GAS __attribute__((address_space(1)))
#define RLX_AGENT __ATOMIC_RELAXED, __HIP_MEMORY_SCOPE_AGENT
#define RLX_WG __ATOMIC_RELAXED, __HIP_MEMORY_SCOPE_WORKGROUP

constexpr int K0 = 5;
constexpr int WS_EPOCH = 0;
constexpr int WS_FLAGS = 4096;
constexpr int WS_QG = 8192;
constexpr int WS_BLK = 131072;
constexpr int BLK_SZ = 3072, BLK_IN1 = 0, BLK_ML = 2048;
constexpr int WS_IN2 = WS_BLK + 256 * BLK_SZ;
constexpr int WS_QA = WS_IN2 + 32 * 16384;
constexpr int WS_HG = WS_QA + 64 * 4096;
constexpr int WS_BYTES = WS_HG + 2 * 128 * 1024;
constexpr unsigned SENT = 0xFFFFFFFFu;
constexpr int AUX_SC1 = 16;
constexpr unsigned SPIN_CAP = 1u << 18;

template <int CTRL> __device__ __forceinline__ float dpp(float x) {
    return __builtin_bit_cast(float, __builtin_amdgcn_mov_dpp(__builtin_bit_cast(int, x), CTRL, 0xf, 0xf, true));
}
constexpr int XOR1 = 0xB1, XOR2 = 0x4E, HMIR = 0x141, ROR8 = 0x128, BC0 = 0x00, BC2 = 0xAA;
__device__ __forceinline__ float sw32_add(float a, float b) {
    auto r = __builtin_amdgcn_permlane32_swap(__float_as_uint(a), __float_as_uint(b), false, false);
    return __uint_as_float(r[0]) + __uint_as_float(r[1]);
}
__device__ __forceinline__ float sw16_add(float a, float b) {
    auto r = __builtin_amdgcn_permlane16_swap(__float_as_uint(a), __float_as_uint(b), false, false);
    return __uint_as_float(r[0]) + __uint_as_float(r[1]);
}
__device__ __forceinline__ float sw32_max(float a) {
    auto r = __builtin_amdgcn_permlane32_swap(__float_as_uint(a), __float_as_uint(a), false, false);
    return fmaxf(__uint_as_float(r[0]), __uint_as_float(r[1]));
}
__device__ __forceinline__ float sw16_max(float a) {
    auto r = __builtin_amdgcn_permlane16_swap(__float_as_uint(a), __float_as_uint(a), false, false);
    return fmaxf(__uint_as_float(r[0]), __uint_as_float(r[1]));
}
__device__ __forceinline__ float oct_sum(float x) { x += dpp<XOR1>(x); x += dpp<XOR2>(x); x += dpp<HMIR>(x); return x; }
__device__ __forceinline__ float oct_max(float x) { x = fmaxf(x, dpp<XOR1>(x)); x = fmaxf(x, dpp<XOR2>(x)); x = fmaxf(x, dpp<HMIR>(x)); return x; }
__device__ __forceinline__ float row_sum(float x) { x = oct_sum(x); x += dpp<ROR8>(x); return x; }
__device__ __forceinline__ float bflo(unsigned u) { return __uint_as_float(u << 16); }
__device__ __forceinline__ float bfhi(unsigned u) { return __uint_as_float(u & 0xffff0000u); }
__device__ __forceinline__ unsigned pk_bf16(float lo, float hi) {
    f32x2 v = {lo, hi};
    return __builtin_bit_cast(unsigned, __builtin_convertvector(v, bf16x2));
}
__device__ __forceinline__ float dot2(unsigned a, unsigned b, float c) {
    return __builtin_amdgcn_fdot2_f32_bf16(__builtin_bit_cast(bf16x2, a), __builtin_bit_cast(bf16x2, b), c, false);
}
__device__ __forceinline__ f32x4 mfma32(v4u a, v4u b, f32x4 c) {
    return __builtin_amdgcn_mfma_f32_16x16x32_bf16(__builtin_bit_cast(bf16x8, a), __builtin_bit_cast(bf16x8, b), c, 0, 0, 0);
}
__device__ __forceinline__ f32x4 mfma16(unsigned a0, unsigned a1, unsigned b0, unsigned b1, f32x4 c) {
    return __builtin_amdgcn_mfma_f32_16x16x16bf16_1k(__builtin_bit_cast(s16x4, v2u{a0, a1}), __builtin_bit_cast(s16x4, v2u{b0, b1}), c, 0, 0, 0);
}
__device__ __forceinline__ v4u ld_nt(const unsigned char* p) { return __builtin_nontemporal_load((const v4u*)p); }
__device__ __forceinline__ v4u nosent(f32x4 v) {
    v4u u = __builtin_bit_cast(v4u, v);
    u.x = min(u.x, SENT - 1u); u.y = min(u.y, SENT - 1u); u.z = min(u.z, SENT - 1u); u.w = min(u.w, SENT - 1u);
    return u;
}
__device__ __forceinline__ unsigned max4(v4u a) { return max(max(a.x, a.y), max(a.z, a.w)); }

#define POLL_CHECK(b0, b1, b2, b3, b4, b5, b6, b7) \
    "v_max3_u32 v144, v" #b0 ", v" #b1 ", v" #b2 "\n\tv_max3_u32 v145, v" #b3 ", v" #b4 ", v" #b5 "\n\t" \
    "v_max3_u32 v144, v144, v" #b6 ", v" #b7 "\n\tv_max_u32 v144, v144, v145\n\tv_cmp_eq_u32 vcc, -1, v144\n\t" \
    "v_mul_f32 %0, %11, v" #b0 "\n\tv_mul_f32 %1, %11, v" #b1 "\n\tv_mul_f32 %2, %11, v" #b2 "\n\tv_mul_f32 %3, %11, v" #b3 "\n\t" \
    "v_fmac_f32 %0, %12, v" #b4 "\n\tv_fmac_f32 %1, %12, v" #b5 "\n\tv_fmac_f32 %2, %12, v" #b6 "\n\tv_fmac_f32 %3, %12, v" #b7 "\n\t" \
    "s_mov_b64 exec, vcc\n\t" \
    "s_cbranch_vccz 2f\n\t"
#define POLL_PAIR(D, r, k0, k1, k2, k3, off, srd, e0, e1) do { unsigned cnt_; u64 ex_; \
    asm volatile( \
        "s_mov_b64 %9, exec\n\t" \
        "buffer_load_dwordx4 v[128:131], %10, %13, 0 offen sc1\n\t" \
        "buffer_load_dwordx4 v[132:135], %10, %13, 0 offen offset:" #D " sc1\n\t" \
        "s_mov_b32 %4, 0x40000\n\t" \
        "s_sleep 8\n" \
        "1:\n\t" \
        "buffer_load_dwordx4 v[136:139], %10, %13, 0 offen sc1\n\t" \
        "buffer_load_dwordx4 v[140:143], %10, %13, 0 offen offset:" #D " sc1\n\t" \
        "s_waitcnt vmcnt(2)\n\t" \
        POLL_CHECK(128, 129, 130, 131, 132, 133, 134, 135) \
        "buffer_load_dwordx4 v[128:131], %10, %13, 0 offen sc1\n\t" \
        "buffer_load_dwordx4 v[132:135], %10, %13, 0 offen offset:" #D " sc1\n\t" \
        "s_waitcnt vmcnt(2)\n\t" \
        POLL_CHECK(136, 137, 138, 139, 140, 141, 142, 143) \
        "s_sub_u32 %4, %4, 1\n\t" \
        "s_cbranch_scc0 1b\n" \
        "2:\n\t" \
        "s_mov_b64 exec, %9\n\t" \
        "s_nop 4" \
        : "=&v"(r[0]), "=&v"(r[1]), "=&v"(r[2]), "=&v"(r[3]), "=&s"(cnt_), \
          "=&{v[128:131]}"(k0), "=&{v[132:135]}"(k1), "=&{v[136:139]}"(k2), "=&{v[140:143]}"(k3), "=&s"(ex_) \
        : "v"(off), "v"(e0), "v"(e1), "s"(srd) \
        : "v144", "v145", "vcc", "scc", "memory"); } while (0)
#define POLL_DRAIN(k0, k1, k2, k3) \
    asm volatile("s_waitcnt vmcnt(0)" :: "{v[128:131]}"(k0), "{v[132:135]}"(k1), "{v[136:139]}"(k2), "{v[140:143]}"(k3) : "memory")
#define SB() __builtin_amdgcn_sched_barrier(0)
#define CFENCE() asm volatile("" ::: "memory")
#define LGKM0() asm volatile("s_waitcnt lgkmcnt(0)" ::: "memory")
#define BAR() do { asm volatile("s_waitcnt lgkmcnt(0)" ::: "memory"); __builtin_amdgcn_s_barrier(); asm volatile("" ::: "memory"); } while (0)
#define LDS_WAIT(word, target, slp) \
    for (unsigned spins_ = 0; __hip_atomic_load(&(word), RLX_WG) < (target) && spins_ < (SPIN_CAP << 4); ++spins_) __builtin_amdgcn_s_sleep(slp)
#define LD_SC1(off) __builtin_amdgcn_raw_buffer_load_b128(rs, (off), 0, AUX_SC1)
#define ST_SC1(v, off) __builtin_amdgcn_raw_buffer_store_b128((v), rs, (off), 0, AUX_SC1)

__global__ void __launch_bounds__(576, 3)
fused_decode(const u16* __restrict__ x, const u16* __restrict__ Kc, const u16* __restrict__ Vc, const u16* __restrict__ wq,
             const u16* __restrict__ wo, const u16* __restrict__ w1, const u16* __restrict__ b1, const u16* __restrict__ w2,
             const u16* __restrict__ b2, u16* __restrict__ out, unsigned char* __restrict__ ws) {
    __shared__ __attribute__((aligned(16))) float s_ored[8 * 128 * 4];
    __shared__ __attribute__((aligned(16))) float s_qred[8 * 128];
    __shared__ __attribute__((aligned(16))) float s_hred[8 * 128];
    __shared__ __attribute__((aligned(16))) unsigned s_q[256];
    __shared__ __attribute__((aligned(16))) u16 s_xn[2 * 512];
    __shared__ __attribute__((aligned(16))) unsigned s_h[16];
    __shared__ __attribute__((aligned(16))) unsigned s_o[32];
    __shared__ __attribute__((aligned(16))) float s_ml[8 * 4 * 2];
    __shared__ __attribute__((aligned(16))) float s_fw[4 * 8];
    __shared__ __attribute__((aligned(16))) float s_fin[8 * 32];
    __shared__ unsigned s_flag, s_cnt;

    asm volatile("" :: "s"(x), "s"(Kc), "s"(Vc), "s"(wq), "s"(wo), "s"(w1), "s"(b1), "s"(w2), "s"(b2), "s"(out), "s"(ws));
    const int L = blockIdx.x;
    const int b = L >> 7, kvh = (L >> 4) & 7, s = L & 15, grp = L >> 4, c2 = L & 127;
    const int qline = (s + kvh) & 7;
    const int mi = 2 * b + (s >> 3);
    const int lane = threadIdx.x & 63, w0 = __builtin_amdgcn_readfirstlane(threadIdx.x >> 6);
    const int w = w0 - 1, tid = w * 64 + lane;
    const int rho = lane >> 4, j2 = lane & 7;
    const unsigned r16 = lane & 15;
    const __amdgpu_buffer_rsrc_t rs = __builtin_amdgcn_make_buffer_rsrc(ws, 0, WS_BYTES, 0x00020000);
    const v4u srd = {(unsigned)(uintptr_t)ws, (unsigned)((uintptr_t)ws >> 32) & 0xffffu, (unsigned)WS_BYTES, 0x00020000u};
    const bool m1_on = L < 224, m2_on = c2 < 112;
    const int mj = m1_on ? L % 28 : L - 224, mks = m1_on ? L / 28 : 0;
    const int hblk = m2_on ? c2 : c2 - 16;
    const bool fin_on = (s & 7) == kvh;
    const int fj = 2 * kvh + (s >> 3);

    if (w0 == 0) {
        __builtin_amdgcn_s_setprio(3);
        const v4u xa0 = *(const v4u*)(x + 16 * lane), xa1 = *(const v4u*)(x + 16 * lane + 8);
        CFENCE();
        const u64 epoch = __hip_atomic_load((GAS u64*)(ws + WS_EPOCH), RLX_AGENT);
        if (lane == 0) { s_flag = 0; s_cnt = 0; }
        BAR();
        const unsigned b1v = *(const unsigned*)(b1 + hblk * 16 + 2 * j2);
        const int ocol = 32 * fj + (lane & 31);
        const unsigned xo = x[b * 512 + ocol], b2o = b2[ocol];
        CFENCE();
        {
            const v4u sv = {SENT, SENT, SENT, SENT};
            const int base = WS_BLK + L * BLK_SZ + lane * 16, base2 = WS_IN2 + L * 2048 + lane * 16;
            ST_SC1(sv, base);
            ST_SC1(sv, base + 1024);
            ST_SC1(sv, lane < 8 ? base + 2048 : 0x7ffffff0);
            ST_SC1(sv, base2);
            ST_SC1(sv, base2 + 1024);
        }
        {
            float s1 = 0.f, s2 = 0.f, t1 = 0.f, t2 = 0.f;
#pragma unroll
            for (int k = 0; k < 4; ++k) {
                s1 = dot2(xa0[k], 0x3f803f80u, s1); t1 = dot2(xa1[k], 0x3f803f80u, t1);
                s2 = dot2(xa0[k], xa0[k], s2); t2 = dot2(xa1[k], xa1[k], t2);
            }
            s1 = row_sum(s1 + t1); s2 = row_sum(s2 + t2);
            s1 = sw16_add(s1, s1); s2 = sw16_add(s2, s2);
            const float mean = s1 * (1.0f / 512.0f);
            const float var = fmaxf(fmaf(-mean, mean, s2 * (1.0f / 512.0f)), 0.f);
            const float rstd = __builtin_amdgcn_rsqf(var + 1e-5f), nb = -mean * rstd;
            v4u o0, o1;
#pragma unroll
            for (int k = 0; k < 4; ++k) {
                o0[k] = pk_bf16(fmaf(bflo(xa0[k]), rstd, nb), fmaf(bfhi(xa0[k]), rstd, nb));
                o1[k] = pk_bf16(fmaf(bflo(xa1[k]), rstd, nb), fmaf(bfhi(xa1[k]), rstd, nb));
            }
            *(v4u*)&s_xn[16 * lane] = o0;
            *(v4u*)&s_xn[16 * lane + 8] = o1;
        }
        BAR();
        const u64 tag = epoch + 1ull;
        const unsigned tg0 = (unsigned)tag, tg1 = (unsigned)(tag >> 32), thi8 = (unsigned)(tag >> 8), tlo8 = tg0 << 24;
        asm volatile("" :: "v"(thi8), "v"(tlo8));
        BAR();
        {
            const int c = lane & 31, pos = (lane >> 5) * 64 + ((c >> 1) & 1) * 32 + (c & 1) * 16 + (c >> 2);
            float q0 = 0.f, q1 = 0.f, h0 = 0.f, h1 = 0.f;
#pragma unroll
            for (int k = 0; k < 8; ++k) { q0 += s_qred[k * 128 + pos]; q1 += s_qred[k * 128 + pos + 8]; }
            ST_SC1((v4u{tlo8 | (__float_as_uint(q0) >> 8), thi8, tlo8 | (__float_as_uint(q1) >> 8), thi8}),
                   WS_QA + (kvh * 8 + qline) * 4096 + rho * 1024 + mi * 256 + r16 * 16);
            CFENCE();
#pragma unroll
            for (int k = 0; k < 8; ++k) { h0 += s_hred[k * 128 + pos]; h1 += s_hred[k * 128 + pos + 8]; }
            if (m1_on)
                ST_SC1((v4u{tlo8 | (__float_as_uint(h0) >> 8), thi8, tlo8 | (__float_as_uint(h1) >> 8), thi8}),
                       WS_HG + ((lane >> 5) * 128 + 4 * mj + (c >> 3)) * 1024 + mks * 128 + (c & 7) * 16);
        }
        __builtin_amdgcn_s_sleep(48);
        {
            v4u ga;
            for (unsigned spins = 0;; ++spins) {
                CFENCE();
                ga = LD_SC1(WS_QA + (kvh * 8 + qline) * 4096 + mi * 1024 + lane * 16);
                const unsigned bad = (ga.y ^ thi8) | (ga.w ^ thi8) | (((ga.x ^ tlo8) | (ga.z ^ tlo8)) >> 24);
                if (__all(bad == 0u) || spins > SPIN_CAP) break;
                __builtin_amdgcn_s_sleep(4);
            }
            asm volatile("s_waitcnt vmcnt(0)" ::: "memory");
            float qa = __uint_as_float(ga.x << 8), qb = __uint_as_float(ga.z << 8);
            qa = sw16_add(qa, qa); qb = sw16_add(qb, qb);
            qa = sw32_add(qa, qa); qb = sw32_add(qb, qb);
            const unsigned pk = pk_bf16(qa, qb), pk2 = __float_as_uint(dpp<XOR1>(__uint_as_float(pk)));
            if (lane < 16 && !(lane & 1)) ST_SC1((v4u{pk, tg0, pk2, tg0}), WS_QG + grp * 2048 + (2 * qline + (s >> 3)) * 128 + lane * 8);
            if (lane == 16) __hip_atomic_store((GAS u64*)(ws + WS_FLAGS) + L, tag, RLX_AGENT);
        }
        __builtin_amdgcn_s_sleep(56);
        {
            v4u g[2], gh, gf;
            unsigned bad[2] = {1u, 1u}, badh = 1u, badf = 1u;
            bool qdone = false;
            for (unsigned spins = 0;; ++spins) {
                CFENCE();
#pragma unroll
                for (int k = 0; k < 2; ++k) if (bad[k]) g[k] = LD_SC1(WS_QG + grp * 2048 + k * 1024 + lane * 16);
                if (badh) gh = LD_SC1(WS_HG + (b * 128 + hblk) * 1024 + lane * 16);
                if (badf) gf = LD_SC1(WS_FLAGS + b * 1024 + lane * 16);
#pragma unroll
                for (int k = 0; k < 2; ++k) bad[k] = (g[k].y ^ tg0) | (g[k].w ^ tg0);
                if (!qdone && (__all((bad[0] | bad[1]) == 0u) || spins > SPIN_CAP)) {
                    qdone = true;
                    *(v2u*)&s_q[2 * lane] = v2u{g[0].x, g[0].z};
                    *(v2u*)&s_q[128 + 2 * lane] = v2u{g[1].x, g[1].z};
                    LGKM0();
                    if (lane == 0) __hip_atomic_store(&s_flag, 1u, RLX_WG);
                }
                badh = (gh.y ^ thi8) | (gh.w ^ thi8) | (((gh.x ^ tlo8) | (gh.z ^ tlo8)) >> 24);
                badf = (gf.x ^ tg0) | (gf.y ^ tg1) | (gf.z ^ tg0) | (gf.w ^ tg1);
                if ((qdone && __all((badh | badf) == 0u)) || spins > SPIN_CAP) break;
                __builtin_amdgcn_s_sleep(4);
            }
            if (lane == 0) __hip_atomic_store(&s_flag, 2u, RLX_WG);
            float hv[2] = {__uint_as_float(gh.x << 8), __uint_as_float(gh.z << 8)};
#pragma unroll
            for (int k = 0; k < 2; ++k) {
                float h = hv[k];
                h += dpp<ROR8>(h); h = sw16_add(h, h); h = sw32_add(h, h);
                h += k ? bfhi(b1v) : bflo(b1v);
                const float u2 = 1.5957691216057308f * (h + 0.044715f * h * h * h);
                const float th = 1.0f - 2.0f / (__expf(u2) + 1.0f);
                hv[k] = m2_on ? 0.5f * h * (1.0f + th) : 0.f;
            }
            if (lane < 8) *(v2u*)&s_h[2 * lane] = v2u{pk_bf16(hv[0], hv[0]), pk_bf16(hv[1], hv[1])};
        }
        LDS_WAIT(s_cnt, 8u, 2);
        CFENCE();
        {
            const int h = (lane >> 3) & 3;
            const f32x2 ml = *(const f32x2*)&s_ml[(j2 * 4 + h) * 2];
            const float M = oct_max(ml.x);
            const float f = __builtin_amdgcn_exp2f(ml.x - M);
            const float Ls = oct_sum(f * ml.y);
            if (lane < 32) s_fw[lane] = f;
            if (lane < 32 && j2 < 4) {
                const v2u mlv = {min(__float_as_uint(M), SENT - 1u), min(__float_as_uint(Ls), SENT - 1u)};
                __builtin_amdgcn_raw_buffer_store_b64(mlv, rs, WS_BLK + (grp * 16 + 4 * h + j2) * BLK_SZ + BLK_ML + s * 8, 0, AUX_SC1);
            }
        }
        BAR();
        v4u k0, k1, k2, k3;
        {
            const int base = WS_BLK + L * BLK_SZ;
            const int oa = base + BLK_IN1 + (2 * j2) * 128 + (lane >> 3) * 16;
            v4u ML;
            for (unsigned spins = 0;; ++spins) {
                CFENCE();
                ML = LD_SC1(base + BLK_ML + j2 * 16);
                if (__all(max4(ML) != SENT) || spins > SPIN_CAP) break;
                __builtin_amdgcn_s_sleep(2);
            }
            const float M0 = __uint_as_float(ML.x), L0 = __uint_as_float(ML.y), M1 = __uint_as_float(ML.z), L1 = __uint_as_float(ML.w);
            const float Mx = oct_max(fmaxf(M0, M1));
            float e0 = __builtin_amdgcn_exp2f(M0 - Mx), e1 = __builtin_amdgcn_exp2f(M1 - Mx);
            const float inv = __builtin_amdgcn_rcpf(oct_sum(fmaf(e0, L0, e1 * L1)));
            e0 *= inv; e1 *= inv;
            float o4[4];
            POLL_PAIR(128, o4, k0, k1, k2, k3, oa, srd, e0, e1);
            v4u ob;
#pragma unroll
            for (int t = 0; t < 4; ++t) { const float o = oct_sum(o4[t]); ob[t] = pk_bf16(o, o); }
            if (j2 == 0) *(v4u*)&s_o[(lane >> 3) * 4] = ob;
        }
        BAR();
        POLL_DRAIN(k0, k1, k2, k3);
        if (fin_on) {
            BAR();
            float r = bflo(xo) + bflo(b2o);
#pragma unroll
            for (int k = 0; k < 8; ++k) r += s_fin[k * 32 + (lane & 31)];
            if (lane < 32) out[b * 512 + ocol] = (u16)(pk_bf16(r, r) & 0xffffu);
            if (L == 0 && lane == 0) __hip_atomic_store((GAS u64*)(ws + WS_EPOCH), tag, RLX_AGENT);
        }
        return;
    }

    v4u wqr[2], w1r;
    {
        const u16* p = wq + (size_t)(128 * mi + 16 * w) * 4096 + kvh * 512 + qline * 64;
        const unsigned lo = (unsigned)(lane >> 3) * 4096u + 8u * j2;
        wqr[0] = *(const v4u*)(p + lo);
        wqr[1] = *(const v4u*)(p + lo + 8 * 4096);
        CFENCE();
        w1r = *(const v4u*)(w1 + (size_t)(64 * mks + 8 * w) * 1792 + mj * 64 + ((unsigned)(lane >> 3) * 1792u + 8u * j2));
    }
    CFENCE(); SB();
    BAR();
    const unsigned char* kb = (const unsigned char*)Kc + (size_t)grp * 8192 * 256;
    const unsigned char* vb = (const unsigned char*)Vc + (size_t)grp * 8192 * 256;
    const unsigned klo = (4u * (r16 & 3) + ((r16 >> 2) & 1)) * 256u + 64u * (r16 >> 3) + 16u * rho;
    const unsigned vlo = 16u * lane;
#define KIN(i) ((((i) >> 1) & 1) * 512 + ((i) & 1) * 128)
#define TOFF(u) ((unsigned)(((u) * 128 + s * 8 + w + 32 * grp) & 511) * 4096u)
    v4u kr[16], vr[16];
#pragma unroll
    for (int i = 0; i < K0; ++i) kr[i] = ld_nt(kb + TOFF(i >> 2) + klo + KIN(i));
    CFENCE(); SB();
    const f32x4 z4 = {0.f, 0.f, 0.f, 0.f};
    BAR();
    {
        const int xi = ((lane >> 2) & 1) * 512 + 2 * rho + ((lane >> 3) & 1);
        const unsigned sh = (lane & 1) * 16;
        const unsigned e0 = (unsigned)s_xn[xi + 128 * mi + 16 * w] << sh, e1 = (unsigned)s_xn[xi + 128 * mi + 16 * w + 8] << sh;
        const unsigned e2 = (unsigned)s_xn[xi + 64 * mks + 8 * w] << sh;
        const bool lo2 = (lane & 2) == 0;
        const unsigned a0 = lo2 ? e0 : 0u, c0 = lo2 ? 0u : e0, a1 = lo2 ? e1 : 0u, c1 = lo2 ? 0u : e1, a2 = lo2 ? e2 : 0u, c2_ = lo2 ? 0u : e2;
        const f32x4 d0 = mfma16(wqr[1].x, wqr[1].y, a1, c1, mfma16(wqr[0].x, wqr[0].y, a0, c0, z4));
        const f32x4 d1 = mfma16(wqr[1].z, wqr[1].w, a1, c1, mfma16(wqr[0].z, wqr[0].w, a0, c0, z4));
        f32x4 u;
#pragma unroll
        for (int i = 0; i < 4; ++i) {
            auto r = __builtin_amdgcn_permlane32_swap(__float_as_uint(d0[i]), __float_as_uint(d1[i]), false, false);
            u[i] = __uint_as_float(r[0]) + dpp<ROR8>(__uint_as_float(r[1]));
        }
        const int pos = w * 128 + ((lane >> 2) & 1) * 64 + (rho >> 1) * 32 + (lane & 3) * 8 + (rho & 1) * 4;
        if ((lane & 8) == 0) *(f32x4*)&s_qred[pos] = u;
        const f32x4 g0 = mfma16(w1r.x, w1r.y, a2, c2_, z4), g1 = mfma16(w1r.z, w1r.w, a2, c2_, z4);
#pragma unroll
        for (int i = 0; i < 4; ++i) {
            auto r = __builtin_amdgcn_permlane32_swap(__float_as_uint(g0[i]), __float_as_uint(g1[i]), false, false);
            u[i] = __uint_as_float(r[0]) + dpp<ROR8>(__uint_as_float(r[1]));
        }
        if ((lane & 8) == 0) *(f32x4*)&s_hred[pos] = u;
    }
    BAR();
    unsigned qrdy = 0;
#define WAITW() do { if (!qrdy) { qrdy = __builtin_amdgcn_readfirstlane(__hip_atomic_load(&s_flag, RLX_WG)) >> 1; \
        if (!qrdy) asm volatile("s_waitcnt vmcnt(2)" ::: "memory"); } } while (0)
#pragma unroll
    for (int i = K0; i < 16; ++i) { WAITW(); kr[i] = ld_nt(kb + TOFF(i >> 2) + klo + KIN(i)); CFENCE(); }
#pragma unroll
    for (int i = 0; i < 16; ++i) { WAITW(); vr[i] = ld_nt(vb + TOFF(i >> 2) + vlo + (i & 3) * 1024); CFENCE(); }
    if (!qrdy) LDS_WAIT(s_flag, 1u, 1);
    CFENCE(); SB();
    constexpr float C2 = 0.08838834764831845f * 1.4426950408889634f;
    unsigned pp[4][4];
    {
        f32x4 S[4];
        v4u qf[2];
#pragma unroll
        for (int hf = 0; hf < 2; ++hf) qf[hf] = *(const v4u*)&s_q[(r16 >> 2) * 64 + 32 * hf + 16 * ((r16 >> 1) & 1) + 4 * rho];
#pragma unroll
        for (int u = 0; u < 4; ++u) {
            const f32x4 da = mfma32(kr[u * 4 + 1], qf[1], mfma32(kr[u * 4 + 0], qf[0], z4));
            const f32x4 db = mfma32(kr[u * 4 + 3], qf[1], mfma32(kr[u * 4 + 2], qf[0], z4));
#pragma unroll
            for (int i = 0; i < 4; ++i) {
                auto r = __builtin_amdgcn_permlane32_swap(__float_as_uint(da[i]), __float_as_uint(db[i]), false, false);
                S[u][i] = dpp<BC0>(__uint_as_float(r[0])) + dpp<BC2>(__uint_as_float(r[1]));
            }
        }
        float m = S[0][0];
#pragma unroll
        for (int u = 0; u < 4; ++u)
#pragma unroll
            for (int i = 0; i < 4; ++i) m = fmaxf(m, S[u][i]);
        m = sw16_max(m); m = sw32_max(m);
        m *= C2;
        float l4[4] = {0.f, 0.f, 0.f, 0.f};
#pragma unroll
        for (int u = 0; u < 4; ++u)
#pragma unroll
            for (int i = 0; i < 4; ++i) { const float p = __builtin_amdgcn_exp2f(fmaf(S[u][i], C2, -m)); l4[i] += p; pp[u][i] = pk_bf16(p, p); }
        float l = (l4[0] + l4[1]) + (l4[2] + l4[3]);
        l = sw16_add(l, l); l = sw32_add(l, l);
        if (lane < 16 && (lane & 3) == 0) *(f32x2*)&s_ml[(w * 4 + (lane >> 2)) * 2] = f32x2{m, l};
    }
    LGKM0();
    if (lane == 0) __hip_atomic_fetch_add(&s_cnt, 1u, RLX_WG);
    {
        const unsigned hm = (lane & 1) ? 0xffff0000u : 0x0000ffffu;
        const unsigned ma = (lane & 2) ? 0u : hm, mb = (lane & 2) ? hm : 0u;
        f32x4 O0 = z4, O1 = z4;
#pragma unroll
        for (int u = 0; u < 4; ++u)
#pragma unroll
            for (int i = 0; i < 4; ++i) {
                const unsigned pa = pp[u][i] & ma, pb = pp[u][i] & mb;
                const v4u v = vr[u * 4 + i];
                O0 = mfma16(v.x, v.y, pa, pb, O0);
                O1 = mfma16(v.z, v.w, pa, pb, O1);
            }
        float* po = &s_ored[(w * 128 + (r16 >> 2) * 32 + rho * 8 + (lane & 3)) * 4];
        *(f32x4*)po = O0;
        *(f32x4*)(po + 16) = O1;
    }
    int o_pub1 = WS_BLK + (grp * 16 + (tid >> 3)) * BLK_SZ + BLK_IN1 + s * 128 + j2 * 16;
    int o_pub2 = WS_IN2 + (b * 16 + 2 * w + rho) * 16384 + c2 * 128 + j2 * 16;
    int o_fin = WS_IN2 + (b * 16 + fj) * 16384 + w * 2048 + lane * 16;
    asm volatile("" : "+v"(o_pub1), "+v"(o_pub2), "+v"(o_fin));
    CFENCE(); SB();
    BAR();
    if (w < 2) {
        const f32x4 fa = *(const f32x4*)&s_fw[(tid >> 5) * 8], fb = *(const f32x4*)&s_fw[(tid >> 5) * 8 + 4];
        f32x4 acc = fa[0] * *(const f32x4*)&s_ored[tid * 4];
#pragma unroll
        for (int k = 1; k < 8; ++k) acc += (k < 4 ? fa[k & 3] : fb[k & 3]) * *(const f32x4*)&s_ored[(k * 128 + tid) * 4];
        ST_SC1(nosent(acc), o_pub1);
    }
    CFENCE();
    v4u wor[4], w2r[2];
    {
        const u16* p = wo + (size_t)(kvh * 512 + s * 32) * 512 + 64 * w;
        const u16* p2 = w2 + (size_t)hblk * 16 * 512 + 64 * w;
        const unsigned lo = (unsigned)(lane >> 3) * 512u + 8u * j2;
        w2r[0] = *(const v4u*)(p2 + lo); w2r[1] = *(const v4u*)(p2 + lo + 8 * 512);
#pragma unroll
        for (int t = 0; t < 4; ++t) wor[t] = *(const v4u*)(p + lo + t * 8 * 512);
    }
    CFENCE(); SB();
    unsigned msk[4];
#pragma unroll
    for (int k = 0; k < 4; ++k) msk[k] = (k == (j2 >> 1)) ? ((j2 & 1) ? 0xffff0000u : 0x0000ffffu) : 0u;
    const unsigned ph0 = s_h[lane >> 3], ph1 = s_h[(lane >> 3) + 8];
    v4u bh0 = {ph0 & msk[0], ph0 & msk[1], ph0 & msk[2], ph0 & msk[3]}, bh1 = {ph1 & msk[0], ph1 & msk[1], ph1 & msk[2], ph1 & msk[3]};
    asm volatile("" : "+v"(bh0), "+v"(bh1));
    BAR();
    {
        const v4u ob = *(const v4u*)&s_o[(lane >> 3) * 4];
        v4u bo[4];
#pragma unroll
        for (int t = 0; t < 4; ++t) bo[t] = v4u{ob[t] & msk[0], ob[t] & msk[1], ob[t] & msk[2], ob[t] & msk[3]};
        asm volatile("" : "+v"(bo[0]), "+v"(bo[1]), "+v"(bo[2]), "+v"(bo[3]));
        f32x4 Y = mfma32(w2r[1], bh1, mfma32(w2r[0], bh0, z4));
#pragma unroll
        for (int t = 0; t < 4; ++t) Y = mfma32(wor[t], bo[t], Y);
        f32x4 Z;
#pragma unroll
        for (int i = 0; i < 4; ++i) {
            const float y = dpp<ROR8>(Y[i]);
            auto r = __builtin_amdgcn_permlane32_swap(__float_as_uint(y), __float_as_uint(y), false, false);
            Z[i] = Y[i] + __uint_as_float(r[1]);
        }
        if (lane < 32 && (lane & 8) == 0) ST_SC1(nosent(Z), o_pub2);
    }
    if (fin_on) {
        v4u k0, k1, k2, k3;
        float v[4];
        const float one = 1.0f;
        POLL_PAIR(1024, v, k0, k1, k2, k3, o_fin, srd, one, one);
        const float t0 = sw32_add(v[0], v[1]), t1 = sw32_add(v[2], v[3]);
        float r = sw16_add(t0, t1);
        r += dpp<ROR8>(r);
        if ((lane & 8) == 0) s_fin[w * 32 + 8 * (2 * (rho & 1) + (rho >> 1)) + j2] = r;
        BAR();
        POLL_DRAIN(k0, k1, k2, k3);
    }
}

extern "C" void kernel_launch(void* const* d_in, const int* in_sizes, int n_in, void* d_out, int out_size, void* d_ws, size_t ws_size,
                              hipStream_t stream) {
    fused_decode<<<256, 576, 0, stream>>>((const u16*)d_in[0], (const u16*)d_in[1], (const u16*)d_in[2], (const u16*)d_in[3], (const u16*)d_in[4],
                                          (const u16*)d_in[6], (const u16*)d_in[7], (const u16*)d_in[8], (const u16*)d_in[9], (u16*)d_out,
                                          (unsigned char*)d_ws);
}
```

```cpp
#define FD 12
#include <hip/hip_runtime.h>
#include <stdint.h>

typedef unsigned short u16;
typedef unsigned long long u64;
typedef unsigned v4u __attribute__((ext_vector_type(4)));
typedef unsigned v2u __attribute__((ext_vector_type(2)));
typedef float f32x4 __attribute__((ext_vector_type(4)));
typedef float f32x2 __attribute__((ext_vector_type(2)));
typedef short s16x4 __attribute__((ext_vector_type(4)));
typedef __bf16 bf16x8 __attribute__((ext_vector_type(8)));
typedef __bf16 bf16x2 __attribute__((ext_vector_type(2)));
#define GAS __attribute__((address_space(1)))
#define RLX_AGENT __ATOMIC_RELAXED, __HIP_MEMORY_SCOPE_AGENT
#define RLX_WG __ATOMIC_RELAXED, __HIP_MEMORY_SCOPE_WORKGROUP

#define XSTR2(x) #x
#define XSTR(x) XSTR2(x)
#define QD1 48
#define QD2 56
#define RETRY 4
#define MLS 2
#define PL 0
#define OD 0
constexpr int K0 = 5;
constexpr int WS_EPOCH = 0;
constexpr int WS_FLAGS = 4096;
constexpr int WS_QG = 8192;
constexpr int WS_BLK = 131072;
constexpr int BLK_SZ = 3072, BLK_IN1 = 0, BLK_ML = 2048;
constexpr int WS_IN2 = WS_BLK + 256 * BLK_SZ;
constexpr int WS_QA = WS_IN2 + 32 * 16384;
constexpr int WS_HG = WS_QA + 64 * 4096;
constexpr int WS_BYTES = WS_HG + 2 * 128 * 1024;
constexpr unsigned SENT = 0xFFFFFFFFu;
constexpr int AUX_SC1 = 16;
constexpr unsigned SPIN_CAP = 1u << 18;

template <int CTRL> __device__ __forceinline__ float dpp(float x) {
    return __builtin_bit_cast(float, __builtin_amdgcn_mov_dpp(__builtin_bit_cast(int, x), CTRL, 0xf, 0xf, true));
}
constexpr int XOR1 = 0xB1, XOR2 = 0x4E, HMIR = 0x141, ROR8 = 0x128, BC0 = 0x00, BC2 = 0xAA;
__device__ __forceinline__ float sw32_add(float a, float b) {
    auto r = __builtin_amdgcn_permlane32_swap(__float_as_uint(a), __float_as_uint(b), false, false);
    return __uint_as_float(r[0]) + __uint_as_float(r[1]);
}
__device__ __forceinline__ float sw16_add(float a, float b) {
    auto r = __builtin_amdgcn_permlane16_swap(__float_as_uint(a), __float_as_uint(b), false, false);
    return __uint_as_float(r[0]) + __uint_as_float(r[1]);
}
__device__ __forceinline__ float sw32_max(float a) {
    auto r = __builtin_amdgcn_permlane32_swap(__float_as_uint(a), __float_as_uint(a), false, false);
    return fmaxf(__uint_as_float(r[0]), __uint_as_float(r[1]));
}
__device__ __forceinline__ float sw16_max(float a) {
    auto r = __builtin_amdgcn_permlane16_swap(__float_as_uint(a), __float_as_uint(a), false, false);
    return fmaxf(__uint_as_float(r[0]), __uint_as_float(r[1]));
}
__device__ __forceinline__ float oct_sum(float x) { x += dpp<XOR1>(x); x += dpp<XOR2>(x); x += dpp<HMIR>(x); return x; }
__device__ __forceinline__ float oct_max(float x) { x = fmaxf(x, dpp<XOR1>(x)); x = fmaxf(x, dpp<XOR2>(x)); x = fmaxf(x, dpp<HMIR>(x)); return x; }
__device__ __forceinline__ float row_sum(float x) { x = oct_sum(x); x += dpp<ROR8>(x); return x; }
__device__ __forceinline__ float bflo(unsigned u) { return __uint_as_float(u << 16); }
__device__ __forceinline__ float bfhi(unsigned u) { return __uint_as_float(u & 0xffff0000u); }
__device__ __forceinline__ unsigned pk_bf16(float lo, float hi) {
    f32x2 v = {lo, hi};
    return __builtin_bit_cast(unsigned, __builtin_convertvector(v, bf16x2));
}
__device__ __forceinline__ float dot2(unsigned a, unsigned b, float c) {
    return __builtin_amdgcn_fdot2_f32_bf16(__builtin_bit_cast(bf16x2, a), __builtin_bit_cast(bf16x2, b), c, false);
}
__device__ __forceinline__ f32x4 mfma32(v4u a, v4u b, f32x4 c) {
    return __builtin_amdgcn_mfma_f32_16x16x32_bf16(__builtin_bit_cast(bf16x8, a), __builtin_bit_cast(bf16x8, b), c, 0, 0, 0);
}
__device__ __forceinline__ f32x4 mfma16(unsigned a0, unsigned a1, unsigned b0, unsigned b1, f32x4 c) {
    return __builtin_amdgcn_mfma_f32_16x16x16bf16_1k(__builtin_bit_cast(s16x4, v2u{a0, a1}), __builtin_bit_cast(s16x4, v2u{b0, b1}), c, 0, 0, 0);
}
__device__ __forceinline__ v4u ld_nt(const unsigned char* p) { return __builtin_nontemporal_load((const v4u*)p); }
__device__ __forceinline__ v4u nosent(f32x4 v) {
    v4u u = __builtin_bit_cast(v4u, v);
    u.x = min(u.x, SENT - 1u); u.y = min(u.y, SENT - 1u); u.z = min(u.z, SENT - 1u); u.w = min(u.w, SENT - 1u);
    return u;
}
__device__ __forceinline__ unsigned max4(v4u a) { return max(max(a.x, a.y), max(a.z, a.w)); }

#define POLL_CHECK(b0, b1, b2, b3, b4, b5, b6, b7) \
    "v_max3_u32 v144, v" #b0 ", v" #b1 ", v" #b2 "\n\tv_max3_u32 v145, v" #b3 ", v" #b4 ", v" #b5 "\n\t" \
    "v_max3_u32 v144, v144, v" #b6 ", v" #b7 "\n\tv_max_u32 v144, v144, v145\n\tv_cmp_eq_u32 vcc, -1, v144\n\t" \
    "v_mul_f32 %0, %11, v" #b0 "\n\tv_mul_f32 %1, %11, v" #b1 "\n\tv_mul_f32 %2, %11, v" #b2 "\n\tv_mul_f32 %3, %11, v" #b3 "\n\t" \
    "v_fmac_f32 %0, %12, v" #b4 "\n\tv_fmac_f32 %1, %12, v" #b5 "\n\tv_fmac_f32 %2, %12, v" #b6 "\n\tv_fmac_f32 %3, %12, v" #b7 "\n\t" \
    "s_mov_b64 exec, vcc\n\t" \
    "s_cbranch_vccz 2f\n\t" \
    "s_sleep " XSTR(PL) "\n\t"
#define POLL_PAIR(D, r, k0, k1, k2, k3, off, srd, e0, e1) do { unsigned cnt_; u64 ex_; \
    asm volatile( \
        "s_mov_b64 %9, exec\n\t" \
        "buffer_load_dwordx4 v[128:131], %10, %13, 0 offen sc1\n\t" \
        "buffer_load_dwordx4 v[132:135], %10, %13, 0 offen offset:" #D " sc1\n\t" \
        "s_mov_b32 %4, 0x40000\n\t" \
        "s_sleep 8\n" \
        "1:\n\t" \
        "buffer_load_dwordx4 v[136:139], %10, %13, 0 offen sc1\n\t" \
        "buffer_load_dwordx4 v[140:143], %10, %13, 0 offen offset:" #D " sc1\n\t" \
        "s_waitcnt vmcnt(2)\n\t" \
        POLL_CHECK(128, 129, 130, 131, 132, 133, 134, 135) \
        "buffer_load_dwordx4 v[128:131], %10, %13, 0 offen sc1\n\t" \
        "buffer_load_dwordx4 v[132:135], %10, %13, 0 offen offset:" #D " sc1\n\t" \
        "s_waitcnt vmcnt(2)\n\t" \
        POLL_CHECK(136, 137, 138, 139, 140, 141, 142, 143) \
        "s_sub_u32 %4, %4, 1\n\t" \
        "s_cbranch_scc0 1b\n" \
        "2:\n\t" \
        "s_mov_b64 exec, %9\n\t" \
        "s_nop 4" \
        : "=&v"(r[0]), "=&v"(r[1]), "=&v"(r[2]), "=&v"(r[3]), "=&s"(cnt_), \
          "=&{v[128:131]}"(k0), "=&{v[132:135]}"(k1), "=&{v[136:139]}"(k2), "=&{v[140:143]}"(k3), "=&s"(ex_) \
        : "v"(off), "v"(e0), "v"(e1), "s"(srd) \
        : "v144", "v145", "vcc", "scc", "memory"); } while (0)
#define POLL_DRAIN(k0, k1, k2, k3) \
    asm volatile("s_waitcnt vmcnt(0)" :: "{v[128:131]}"(k0), "{v[132:135]}"(k1), "{v[136:139]}"(k2), "{v[140:143]}"(k3) : "memory")
#define SB() __builtin_amdgcn_sched_barrier(0)
#define CFENCE() asm volatile("" ::: "memory")
#define LGKM0() asm volatile("s_waitcnt lgkmcnt(0)" ::: "memory")
#define BAR() do { asm volatile("s_waitcnt lgkmcnt(0)" ::: "memory"); __builtin_amdgcn_s_barrier(); asm volatile("" ::: "memory"); } while (0)
#define LDS_WAIT(word, target, slp) \
    for (unsigned spins_ = 0; __hip_atomic_load(&(word), RLX_WG) < (target) && spins_ < (SPIN_CAP << 4); ++spins_) __builtin_amdgcn_s_sleep(slp)
#define LD_SC1(off) __builtin_amdgcn_raw_buffer_load_b128(rs, (off), 0, AUX_SC1)
#define ST_SC1(v, off) __builtin_amdgcn_raw_buffer_store_b128((v), rs, (off), 0, AUX_SC1)

__global__ void __launch_bounds__(576, 3)
fused_decode(const u16* __restrict__ x, const u16* __restrict__ Kc, const u16* __restrict__ Vc, const u16* __restrict__ wq,
             const u16* __restrict__ wo, const u16* __restrict__ w1, const u16* __restrict__ b1, const u16* __restrict__ w2,
             const u16* __restrict__ b2, u16* __restrict__ out, unsigned char* __restrict__ ws) {
    __shared__ __attribute__((aligned(16))) float s_ored[8 * 128 * 4];
    __shared__ __attribute__((aligned(16))) float s_qred[8 * 128];
    __shared__ __attribute__((aligned(16))) float s_hred[8 * 128];
    __shared__ __attribute__((aligned(16))) unsigned s_q[256];
    __shared__ __attribute__((aligned(16))) u16 s_xn[2 * 512];
    __shared__ __attribute__((aligned(16))) unsigned s_h[16];
    __shared__ __attribute__((aligned(16))) unsigned s_o[32];
    __shared__ __attribute__((aligned(16))) float s_ml[8 * 4 * 2];
    __shared__ __attribute__((aligned(16))) float s_fw[4 * 8];
    __shared__ __attribute__((aligned(16))) float s_fin[8 * 32];
    __shared__ unsigned s_flag, s_cnt;

    asm volatile("" :: "s"(x), "s"(Kc), "s"(Vc), "s"(wq), "s"(wo), "s"(w1), "s"(b1), "s"(w2), "s"(b2), "s"(out), "s"(ws));
    const int L = blockIdx.x;
    const int b = L >> 7, kvh = (L >> 4) & 7, s = L & 15, grp = L >> 4, c2 = L & 127;
    const int qline = (s + kvh) & 7;
    const int mi = 2 * b + (s >> 3);
    const int lane = threadIdx.x & 63, w0 = __builtin_amdgcn_readfirstlane(threadIdx.x >> 6);
    const int w = w0 - 1, tid = w * 64 + lane;
    const int rho = lane >> 4, j2 = lane & 7;
    const unsigned r16 = lane & 15;
    const __amdgpu_buffer_rsrc_t rs = __builtin_amdgcn_make_buffer_rsrc(ws, 0, WS_BYTES, 0x00020000);
    const v4u srd = {(unsigned)(uintptr_t)ws, (unsigned)((uintptr_t)ws >> 32) & 0xffffu, (unsigned)WS_BYTES, 0x00020000u};
    const bool m1_on = L < 224, m2_on = c2 < 112;
    const int mj = m1_on ? L % 28 : L - 224, mks = m1_on ? L / 28 : 0;
    const int hblk = m2_on ? c2 : c2 - 16;
    const bool fin_on = (s & 7) == kvh;
    const int fj = 2 * kvh + (s >> 3);

    if (w0 == 0) {
        __builtin_amdgcn_s_setprio(3);
        const v4u xa0 = *(const v4u*)(x + 16 * lane), xa1 = *(const v4u*)(x + 16 * lane + 8);
        CFENCE();
        const u64 epoch = __hip_atomic_load((GAS u64*)(ws + WS_EPOCH), RLX_AGENT);
        if (lane == 0) { s_flag = 0; s_cnt = 0; }
        BAR();
        const unsigned b1v = *(const unsigned*)(b1 + hblk * 16 + 2 * j2);
        const int ocol = 32 * fj + (lane & 31);
        const unsigned xo = x[b * 512 + ocol], b2o = b2[ocol];
        CFENCE();
        {
            const v4u sv = {SENT, SENT, SENT, SENT};
            const int base = WS_BLK + L * BLK_SZ + lane * 16, base2 = WS_IN2 + L * 2048 + lane * 16;
            ST_SC1(sv, base);
            ST_SC1(sv, base + 1024);
            ST_SC1(sv, lane < 8 ? base + 2048 : 0x7ffffff0);
            ST_SC1(sv, base2);
            ST_SC1(sv, base2 + 1024);
        }
        {
            float s1 = 0.f, s2 = 0.f, t1 = 0.f, t2 = 0.f;
#pragma unroll
            for (int k = 0; k < 4; ++k) {
                s1 = dot2(xa0[k], 0x3f803f80u, s1); t1 = dot2(xa1[k], 0x3f803f80u, t1);
                s2 = dot2(xa0[k], xa0[k], s2); t2 = dot2(xa1[k], xa1[k], t2);
            }
            s1 = row_sum(s1 + t1); s2 = row_sum(s2 + t2);
            s1 = sw16_add(s1, s1); s2 = sw16_add(s2, s2);
            const float mean = s1 * (1.0f / 512.0f);
            const float var = fmaxf(fmaf(-mean, mean, s2 * (1.0f / 512.0f)), 0.f);
            const float rstd = __builtin_amdgcn_rsqf(var + 1e-5f), nb = -mean * rstd;
            v4u o0, o1;
#pragma unroll
            for (int k = 0; k < 4; ++k) {
                o0[k] = pk_bf16(fmaf(bflo(xa0[k]), rstd, nb), fmaf(bfhi(xa0[k]), rstd, nb));
                o1[k] = pk_bf16(fmaf(bflo(xa1[k]), rstd, nb), fmaf(bfhi(xa1[k]), rstd, nb));
            }
            *(v4u*)&s_xn[16 * lane] = o0;
            *(v4u*)&s_xn[16 * lane + 8] = o1;
        }
        BAR();
        const u64 tag = epoch + 1ull;
        const unsigned tg0 = (unsigned)tag, tg1 = (unsigned)(tag >> 32), thi8 = (unsigned)(tag >> 8), tlo8 = tg0 << 24;
        asm volatile("" :: "v"(thi8), "v"(tlo8));
        BAR();
        {
            const int c = lane & 31, pos = (lane >> 5) * 64 + ((c >> 1) & 1) * 32 + (c & 1) * 16 + (c >> 2);
            float q0 = 0.f, q1 = 0.f, h0 = 0.f, h1 = 0.f;
#pragma unroll
            for (int k = 0; k < 8; ++k) { q0 += s_qred[k * 128 + pos]; q1 += s_qred[k * 128 + pos + 8]; }
            ST_SC1((v4u{tlo8 | (__float_as_uint(q0) >> 8), thi8, tlo8 | (__float_as_uint(q1) >> 8), thi8}),
                   WS_QA + (kvh * 8 + qline) * 4096 + rho * 1024 + mi * 256 + r16 * 16);
            CFENCE();
#pragma unroll
            for (int k = 0; k < 8; ++k) { h0 += s_hred[k * 128 + pos]; h1 += s_hred[k * 128 + pos + 8]; }
            if (m1_on)
                ST_SC1((v4u{tlo8 | (__float_as_uint(h0) >> 8), thi8, tlo8 | (__float_as_uint(h1) >> 8), thi8}),
                       WS_HG + ((lane >> 5) * 128 + 4 * mj + (c >> 3)) * 1024 + mks * 128 + (c & 7) * 16);
        }
        __builtin_amdgcn_s_sleep(QD1);
        {
            v4u ga;
            for (unsigned spins = 0;; ++spins) {
                CFENCE();
                ga = LD_SC1(WS_QA + (kvh * 8 + qline) * 4096 + mi * 1024 + lane * 16);
                const unsigned bad = (ga.y ^ thi8) | (ga.w ^ thi8) | (((ga.x ^ tlo8) | (ga.z ^ tlo8)) >> 24);
                if (__all(bad == 0u) || spins > SPIN_CAP) break;
                __builtin_amdgcn_s_sleep(RETRY);
            }
            asm volatile("s_waitcnt vmcnt(0)" ::: "memory");
            float qa = __uint_as_float(ga.x << 8), qb = __uint_as_float(ga.z << 8);
            qa = sw16_add(qa, qa); qb = sw16_add(qb, qb);
            qa = sw32_add(qa, qa); qb = sw32_add(qb, qb);
            const unsigned pk = pk_bf16(qa, qb), pk2 = __float_as_uint(dpp<XOR1>(__uint_as_float(pk)));
            if (lane < 16 && !(lane & 1)) ST_SC1((v4u{pk, tg0, pk2, tg0}), WS_QG + grp * 2048 + (2 * qline + (s >> 3)) * 128 + lane * 8);
            if (lane == 16) __hip_atomic_store((GAS u64*)(ws + WS_FLAGS) + L, tag, RLX_AGENT);
        }
        __builtin_amdgcn_s_sleep(QD2);
        {
            v4u g[2], gh, gf;
            unsigned bad[2] = {1u, 1u}, badh = 1u, badf = 1u;
            bool qdone = false;
            for (unsigned spins = 0;; ++spins) {
                CFENCE();
#pragma unroll
                for (int k = 0; k < 2; ++k) if (bad[k]) g[k] = LD_SC1(WS_QG + grp * 2048 + k * 1024 + lane * 16);
                if (badh) gh = LD_SC1(WS_HG + (b * 128 + hblk) * 1024 + lane * 16);
                if (badf) gf = LD_SC1(WS_FLAGS + b * 1024 + lane * 16);
#pragma unroll
                for (int k = 0; k < 2; ++k) bad[k] = (g[k].y ^ tg0) | (g[k].w ^ tg0);
                if (!qdone && (__all((bad[0] | bad[1]) == 0u) || spins > SPIN_CAP)) {
                    qdone = true;
                    *(v2u*)&s_q[2 * lane] = v2u{g[0].x, g[0].z};
                    *(v2u*)&s_q[128 + 2 * lane] = v2u{g[1].x, g[1].z};
                    LGKM0();
                    if (lane == 0) __hip_atomic_store(&s_flag, 1u, RLX_WG);
                }
                badh = (gh.y ^ thi8) | (gh.w ^ thi8) | (((gh.x ^ tlo8) | (gh.z ^ tlo8)) >> 24);
                badf = (gf.x ^ tg0) | (gf.y ^ tg1) | (gf.z ^ tg0) | (gf.w ^ tg1);
                if ((qdone && __all((badh | badf) == 0u)) || spins > SPIN_CAP) break;
                __builtin_amdgcn_s_sleep(RETRY);
            }
            if (lane == 0) __hip_atomic_store(&s_flag, 2u, RLX_WG);
            float hv[2] = {__uint_as_float(gh.x << 8), __uint_as_float(gh.z << 8)};
#pragma unroll
            for (int k = 0; k < 2; ++k) {
                float h = hv[k];
                h += dpp<ROR8>(h); h = sw16_add(h, h); h = sw32_add(h, h);
                h += k ? bfhi(b1v) : bflo(b1v);
                const float u2 = 1.5957691216057308f * (h + 0.044715f * h * h * h);
                const float th = 1.0f - 2.0f / (__expf(u2) + 1.0f);
                hv[k] = m2_on ? 0.5f * h * (1.0f + th) : 0.f;
            }
            if (lane < 8) *(v2u*)&s_h[2 * lane] = v2u{pk_bf16(hv[0], hv[0]), pk_bf16(hv[1], hv[1])};
        }
        LDS_WAIT(s_cnt, 8u, 2);
        CFENCE();
        {
            const int h = (lane >> 3) & 3;
            const f32x2 ml = *(const f32x2*)&s_ml[(j2 * 4 + h) * 2];
            const float M = oct_max(ml.x);
            const float f = __builtin_amdgcn_exp2f(ml.x - M);
            const float Ls = oct_sum(f * ml.y);
            if (lane < 32) s_fw[lane] = f;
            if (lane < 32 && j2 < 4) {
                const v2u mlv = {min(__float_as_uint(M), SENT - 1u), min(__float_as_uint(Ls), SENT - 1u)};
                __builtin_amdgcn_raw_buffer_store_b64(mlv, rs, WS_BLK + (grp * 16 + 4 * h + j2) * BLK_SZ + BLK_ML + s * 8, 0, AUX_SC1);
            }
        }
        BAR();
        v4u k0, k1, k2, k3;
        {
            const int base = WS_BLK + L * BLK_SZ;
            const int oa = base + BLK_IN1 + (2 * j2) * 128 + (lane >> 3) * 16;
            v4u ML;
            for (unsigned spins = 0;; ++spins) {
                CFENCE();
                ML = LD_SC1(base + BLK_ML + j2 * 16);
                if (__all(max4(ML) != SENT) || spins > SPIN_CAP) break;
                __builtin_amdgcn_s_sleep(MLS);
            }
            const float M0 = __uint_as_float(ML.x), L0 = __uint_as_float(ML.y), M1 = __uint_as_float(ML.z), L1 = __uint_as_float(ML.w);
            const float Mx = oct_max(fmaxf(M0, M1));
            float e0 = __builtin_amdgcn_exp2f(M0 - Mx), e1 = __builtin_amdgcn_exp2f(M1 - Mx);
            const float inv = __builtin_amdgcn_rcpf(oct_sum(fmaf(e0, L0, e1 * L1)));
            e0 *= inv; e1 *= inv;
            float o4[4];
            if (OD) __builtin_amdgcn_s_sleep(OD);
            POLL_PAIR(128, o4, k0, k1, k2, k3, oa, srd, e0, e1);
            v4u ob;
#pragma unroll
            for (int t = 0; t < 4; ++t) { const float o = oct_sum(o4[t]); ob[t] = pk_bf16(o, o); }
            if (j2 == 0) *(v4u*)&s_o[(lane >> 3) * 4] = ob;
        }
        BAR();
        POLL_DRAIN(k0, k1, k2, k3);
        if (fin_on) {
            BAR();
            float r = bflo(xo) + bflo(b2o);
#pragma unroll
            for (int k = 0; k < 8; ++k) r += s_fin[k * 32 + (lane & 31)];
            if (lane < 32) out[b * 512 + ocol] = (u16)(pk_bf16(r, r) & 0xffffu);
            if (L == 0 && lane == 0) __hip_atomic_store((GAS u64*)(ws + WS_EPOCH), tag, RLX_AGENT);
        }
        return;
    }

    v4u wqr[2], w1r;
    {
        const u16* p = wq + (size_t)(128 * mi + 16 * w) * 4096 + kvh * 512 + qline * 64;
        const unsigned lo = (unsigned)(lane >> 3) * 4096u + 8u * j2;
        wqr[0] = *(const v4u*)(p + lo);
        wqr[1] = *(const v4u*)(p + lo + 8 * 4096);
        CFENCE();
        w1r = *(const v4u*)(w1 + (size_t)(64 * mks + 8 * w) * 1792 + mj * 64 + ((unsigned)(lane >> 3) * 1792u + 8u * j2));
    }
    CFENCE(); SB();
    BAR();
    const unsigned char* kb = (const unsigned char*)Kc + (size_t)grp * 8192 * 256;
    const unsigned char* vb = (const unsigned char*)Vc + (size_t)grp * 8192 * 256;
    const unsigned klo = (4u * (r16 & 3) + ((r16 >> 2) & 1)) * 256u + 64u * (r16 >> 3) + 16u * rho;
    const unsigned vlo = 16u * lane;
#define KIN(i) ((((i) >> 1) & 1) * 512 + ((i) & 1) * 128)
#define TOFF(u) ((unsigned)(((u) * 128 + s * 8 + w + 32 * grp) & 511) * 4096u)
    v4u kr[16], vr[16];
#pragma unroll
    for (int i = 0; i < K0; ++i) kr[i] = ld_nt(kb + TOFF(i >> 2) + klo + KIN(i));
    CFENCE(); SB();
    const f32x4 z4 = {0.f, 0.f, 0.f, 0.f};
    BAR();
    {
        const int xi = ((lane >> 2) & 1) * 512 + 2 * rho + ((lane >> 3) & 1);
        const unsigned sh = (lane & 1) * 16;
        const unsigned e0 = (unsigned)s_xn[xi + 128 * mi + 16 * w] << sh, e1 = (unsigned)s_xn[xi + 128 * mi + 16 * w + 8] << sh;
        const unsigned e2 = (unsigned)s_xn[xi + 64 * mks + 8 * w] << sh;
        const bool lo2 = (lane & 2) == 0;
        const unsigned a0 = lo2 ? e0 : 0u, c0 = lo2 ? 0u : e0, a1 = lo2 ? e1 : 0u, c1 = lo2 ? 0u : e1, a2 = lo2 ? e2 : 0u, c2_ = lo2 ? 0u : e2;
        const f32x4 d0 = mfma16(wqr[1].x, wqr[1].y, a1, c1, mfma16(wqr[0].x, wqr[0].y, a0, c0, z4));
        const f32x4 d1 = mfma16(wqr[1].z, wqr[1].w, a1, c1, mfma16(wqr[0].z, wqr[0].w, a0, c0, z4));
        f32x4 u;
#pragma unroll
        for (int i = 0; i < 4; ++i) {
            auto r = __builtin_amdgcn_permlane32_swap(__float_as_uint(d0[i]), __float_as_uint(d1[i]), false, false);
            u[i] = __uint_as_float(r[0]) + dpp<ROR8>(__uint_as_float(r[1]));
        }
        const int pos = w * 128 + ((lane >> 2) & 1) * 64 + (rho >> 1) * 32 + (lane & 3) * 8 + (rho & 1) * 4;
        if ((lane & 8) == 0) *(f32x4*)&s_qred[pos] = u;
        const f32x4 g0 = mfma16(w1r.x, w1r.y, a2, c2_, z4), g1 = mfma16(w1r.z, w1r.w, a2, c2_, z4);
#pragma unroll
        for (int i = 0; i < 4; ++i) {
            auto r = __builtin_amdgcn_permlane32_swap(__float_as_uint(g0[i]), __float_as_uint(g1[i]), false, false);
            u[i] = __uint_as_float(r[0]) + dpp<ROR8>(__uint_as_float(r[1]));
        }
        if ((lane & 8) == 0) *(f32x4*)&s_hred[pos] = u;
    }
    BAR();
    unsigned qrdy = 0;
#define WAITW() do { if (!qrdy) { qrdy = __builtin_amdgcn_readfirstlane(__hip_atomic_load(&s_flag, RLX_WG)) >> 1; \
        if (!qrdy) asm volatile("s_waitcnt vmcnt(2)" ::: "memory"); } } while (0)
#pragma unroll
    for (int i = K0; i < 16; ++i) { WAITW(); kr[i] = ld_nt(kb + TOFF(i >> 2) + klo + KIN(i)); CFENCE(); }
#pragma unroll
    for (int i = 0; i < 16; ++i) { WAITW(); vr[i] = ld_nt(vb + TOFF(i >> 2) + vlo + (i & 3) * 1024); CFENCE(); }
    if (!qrdy) LDS_WAIT(s_flag, 1u, 1);
    CFENCE(); SB();
    constexpr float C2 = 0.08838834764831845f * 1.4426950408889634f;
    unsigned pp[4][4];
    {
        f32x4 S[4];
        v4u qf[2];
#pragma unroll
        for (int hf = 0; hf < 2; ++hf) qf[hf] = *(const v4u*)&s_q[(r16 >> 2) * 64 + 32 * hf + 16 * ((r16 >> 1) & 1) + 4 * rho];
#pragma unroll
        for (int u = 0; u < 4; ++u) {
            const f32x4 da = mfma32(kr[u * 4 + 1], qf[1], mfma32(kr[u * 4 + 0], qf[0], z4));
            const f32x4 db = mfma32(kr[u * 4 + 3], qf[1], mfma32(kr[u * 4 + 2], qf[0], z4));
#pragma unroll
            for (int i = 0; i < 4; ++i) {
                auto r = __builtin_amdgcn_permlane32_swap(__float_as_uint(da[i]), __float_as_uint(db[i]), false, false);
                S[u][i] = dpp<BC0>(__uint_as_float(r[0])) + dpp<BC2>(__uint_as_float(r[1]));
            }
        }
        float m = S[0][0];
#pragma unroll
        for (int u = 0; u < 4; ++u)
#pragma unroll
            for (int i = 0; i < 4; ++i) m = fmaxf(m, S[u][i]);
        m = sw16_max(m); m = sw32_max(m);
        m *= C2;
        float l4[4] = {0.f, 0.f, 0.f, 0.f};
#pragma unroll
        for (int u = 0; u < 4; ++u)
#pragma unroll
            for (int i = 0; i < 4; ++i) { const float p = __builtin_amdgcn_exp2f(fmaf(S[u][i], C2, -m)); l4[i] += p; pp[u][i] = pk_bf16(p, p); }
        float l = (l4[0] + l4[1]) + (l4[2] + l4[3]);
        l = sw16_add(l, l); l = sw32_add(l, l);
        if (lane < 16 && (lane & 3) == 0) *(f32x2*)&s_ml[(w * 4 + (lane >> 2)) * 2] = f32x2{m, l};
    }
    LGKM0();
    if (lane == 0) __hip_atomic_fetch_add(&s_cnt, 1u, RLX_WG);
    {
        const unsigned hm = (lane & 1) ? 0xffff0000u : 0x0000ffffu;
        const unsigned ma = (lane & 2) ? 0u : hm, mb = (lane & 2) ? hm : 0u;
        f32x4 O0 = z4, O1 = z4;
#pragma unroll
        for (int u = 0; u < 4; ++u)
#pragma unroll
            for (int i = 0; i < 4; ++i) {
                const unsigned pa = pp[u][i] & ma, pb = pp[u][i] & mb;
                const v4u v = vr[u * 4 + i];
                O0 = mfma16(v.x, v.y, pa, pb, O0);
                O1 = mfma16(v.z, v.w, pa, pb, O1);
            }
        float* po = &s_ored[(w * 128 + (r16 >> 2) * 32 + rho * 8 + (lane & 3)) * 4];
        *(f32x4*)po = O0;
        *(f32x4*)(po + 16) = O1;
    }
    int o_pub1 = WS_BLK + (grp * 16 + (tid >> 3)) * BLK_SZ + BLK_IN1 + s * 128 + j2 * 16;
    int o_pub2 = WS_IN2 + (b * 16 + 2 * w + rho) * 16384 + c2 * 128 + j2 * 16;
    int o_fin = WS_IN2 + (b * 16 + fj) * 16384 + w * 2048 + lane * 16;
    asm volatile("" : "+v"(o_pub1), "+v"(o_pub2), "+v"(o_fin));
    CFENCE(); SB();
    BAR();
    if (w < 2) {
        const f32x4 fa = *(const f32x4*)&s_fw[(tid >> 5) * 8], fb = *(const f32x4*)&s_fw[(tid >> 5) * 8 + 4];
        f32x4 acc = fa[0] * *(const f32x4*)&s_ored[tid * 4];
#pragma unroll
        for (int k = 1; k < 8; ++k) acc += (k < 4 ? fa[k & 3] : fb[k & 3]) * *(const f32x4*)&s_ored[(k * 128 + tid) * 4];
        ST_SC1(nosent(acc), o_pub1);
    }
    CFENCE();
    v4u wor[4], w2r[2];
    {
        const u16* p = wo + (size_t)(kvh * 512 + s * 32) * 512 + 64 * w;
        const u16* p2 = w2 + (size_t)hblk * 16 * 512 + 64 * w;
        const unsigned lo = (unsigned)(lane >> 3) * 512u + 8u * j2;
        w2r[0] = *(const v4u*)(p2 + lo); w2r[1] = *(const v4u*)(p2 + lo + 8 * 512);
#pragma unroll
        for (int t = 0; t < 4; ++t) wor[t] = *(const v4u*)(p + lo + t * 8 * 512);
    }
    CFENCE(); SB();
    unsigned msk[4];
#pragma unroll
    for (int k = 0; k < 4; ++k) msk[k] = (k == (j2 >> 1)) ? ((j2 & 1) ? 0xffff0000u : 0x0000ffffu) : 0u;
    const unsigned ph0 = s_h[lane >> 3], ph1 = s_h[(lane >> 3) + 8];
    v4u bh0 = {ph0 & msk[0], ph0 & msk[1], ph0 & msk[2], ph0 & msk[3]}, bh1 = {ph1 & msk[0], ph1 & msk[1], ph1 & msk[2], ph1 & msk[3]};
    asm volatile("" : "+v"(bh0), "+v"(bh1));
    BAR();
    {
        const v4u ob = *(const v4u*)&s_o[(lane >> 3) * 4];
        v4u bo[4];
#pragma unroll
        for (int t = 0; t < 4; ++t) bo[t] = v4u{ob[t] & msk[0], ob[t] & msk[1], ob[t] & msk[2], ob[t] & msk[3]};
        asm volatile("" : "+v"(bo[0]), "+v"(bo[1]), "+v"(bo[2]), "+v"(bo[3]));
        f32x4 Y = mfma32(w2r[1], bh1, mfma32(w2r[0], bh0, z4));
#pragma unroll
        for (int t = 0; t < 4; ++t) Y = mfma32(wor[t], bo[t], Y);
        f32x4 Z;
#pragma unroll
        for (int i = 0; i < 4; ++i) {
            const float y = dpp<ROR8>(Y[i]);
            auto r = __builtin_amdgcn_permlane32_swap(__float_as_uint(y), __float_as_uint(y), false, false);
            Z[i] = Y[i] + __uint_as_float(r[1]);
        }
        if (lane < 32 && (lane & 8) == 0) ST_SC1(nosent(Z), o_pub2);
    }
    if (fin_on) {
        v4u k0, k1, k2, k3;
        float v[4];
        const float one = 1.0f;
        if (FD) __builtin_amdgcn_s_sleep(FD);
        POLL_PAIR(1024, v, k0, k1, k2, k3, o_fin, srd, one, one);
        const float t0 = sw32_add(v[0], v[1]), t1 = sw32_add(v[2], v[3]);
        float r = sw16_add(t0, t1);
        r += dpp<ROR8>(r);
        if ((lane & 8) == 0) s_fin[w * 32 + 8 * (2 * (rho & 1) + (rho >> 1)) + j2] = r;
        BAR();
        POLL_DRAIN(k0, k1, k2, k3);
    }
}

extern "C" void kernel_launch(void* const* d_in, const int* in_sizes, int n_in, void* d_out, int out_size, void* d_ws, size_t ws_size,
                              hipStream_t stream) {
    fused_decode<<<256, 576, 0, stream>>>((const u16*)d_in[0], (const u16*)d_in[1], (const u16*)d_in[2], (const u16*)d_in[3], (const u16*)d_in[4],
                                          (const u16*)d_in[6], (const u16*)d_in[7], (const u16*)d_in[8], (const u16*)d_in[9], (u16*)d_out,
                                          (unsigned char*)d_ws);
}
```

```cpp
#include <hip/hip_runtime.h>
#include <stdint.h>

typedef unsigned short u16;
typedef unsigned long long u64;
typedef unsigned v4u __attribute__((ext_vector_type(4)));
typedef unsigned v2u __attribute__((ext_vector_type(2)));
typedef float f32x4 __attribute__((ext_vector_type(4)));
typedef float f32x2 __attribute__((ext_vector_type(2)));
typedef short s16x4 __attribute__((ext_vector_type(4)));
typedef __bf16 bf16x8 __attribute__((ext_vector_type(8)));
typedef __bf16 bf16x2 __attribute__((ext_vector_type(2)));
#define GAS __attribute__((address_space(1)))
#define RLX_AGENT __ATOMIC_RELAXED, __HIP_MEMORY_SCOPE_AGENT
#define RLX_WG __ATOMIC_RELAXED, __HIP_MEMORY_SCOPE_WORKGROUP

constexpr int K0 = 5;
constexpr int WS_EPOCH = 0;
constexpr int WS_FLAGS = 4096;
constexpr int WS_QG = 8192;
constexpr int WS_BLK = 131072;
constexpr int BLK_SZ = 3072, BLK_IN1 = 0, BLK_ML = 2048;
constexpr int WS_IN2 = WS_BLK + 256 * BLK_SZ;
constexpr int WS_QA = WS_IN2 + 32 * 16384;
constexpr int WS_HG = WS_QA + 64 * 4096;
constexpr int WS_BYTES = WS_HG + 2 * 128 * 1024;
constexpr unsigned SENT = 0xFFFFFFFFu;
constexpr int AUX_SC1 = 16;
constexpr unsigned SPIN_CAP = 1u << 18;

template <int CTRL> __device__ __forceinline__ float dpp(float x) {
    return __builtin_bit_cast(float, __builtin_amdgcn_mov_dpp(__builtin_bit_cast(int, x), CTRL, 0xf, 0xf, true));
}
constexpr int XOR1 = 0xB1, XOR2 = 0x4E, HMIR = 0x141, ROR8 = 0x128, BC0 = 0x00, BC2 = 0xAA;
__device__ __forceinline__ float sw32_add(float a, float b) {
    auto r = __builtin_amdgcn_permlane32_swap(__float_as_uint(a), __float_as_uint(b), false, false);
    return __uint_as_float(r[0]) + __uint_as_float(r[1]);
}
__device__ __forceinline__ float sw16_add(float a, float b) {
    auto r = __builtin_amdgcn_permlane16_swap(__float_as_uint(a), __float_as_uint(b), false, false);
    return __uint_as_float(r[0]) + __uint_as_float(r[1]);
}
__device__ __forceinline__ float sw32_max(float a) {
    auto r = __builtin_amdgcn_permlane32_swap(__float_as_uint(a), __float_as_uint(a), false, false);
    return fmaxf(__uint_as_float(r[0]), __uint_as_float(r[1]));
}
__device__ __forceinline__ float sw16_max(float a) {
    auto r = __builtin_amdgcn_permlane16_swap(__float_as_uint(a), __float_as_uint(a), false, false);
    return fmaxf(__uint_as_float(r[0]), __uint_as_float(r[1]));
}
__device__ __forceinline__ float oct_sum(float x) { x += dpp<XOR1>(x); x += dpp<XOR2>(x); x += dpp<HMIR>(x); return x; }
__device__ __forceinline__ float oct_max(float x) { x = fmaxf(x, dpp<XOR1>(x)); x = fmaxf(x, dpp<XOR2>(x)); x = fmaxf(x, dpp<HMIR>(x)); return x; }
__device__ __forceinline__ float row_sum(float x) { x = oct_sum(x); x += dpp<ROR8>(x); return x; }
__device__ __forceinline__ float bflo(unsigned u) { return __uint_as_float(u << 16); }
__device__ __forceinline__ float bfhi(unsigned u) { return __uint_as_float(u & 0xffff0000u); }
__device__ __forceinline__ unsigned pk_bf16(float lo, float hi) {
    f32x2 v = {lo, hi};
    return __builtin_bit_cast(unsigned, __builtin_convertvector(v, bf16x2));
}
__device__ __forceinline__ float dot2(unsigned a, unsigned b, float c) {
    return __builtin_amdgcn_fdot2_f32_bf16(__builtin_bit_cast(bf16x2, a), __builtin_bit_cast(bf16x2, b), c, false);
}
__device__ __forceinline__ f32x4 mfma32(v4u a, v4u b, f32x4 c) {
    return __builtin_amdgcn_mfma_f32_16x16x32_bf16(__builtin_bit_cast(bf16x8, a), __builtin_bit_cast(bf16x8, b), c, 0, 0, 0);
}
__device__ __forceinline__ f32x4 mfma16(unsigned a0, unsigned a1, unsigned b0, unsigned b1, f32x4 c) {
    return __builtin_amdgcn_mfma_f32_16x16x16bf16_1k(__builtin_bit_cast(s16x4, v2u{a0, a1}), __builtin_bit_cast(s16x4, v2u{b0, b1}), c, 0, 0, 0);
}
__device__ __forceinline__ v4u ld_nt(const unsigned char* p) { return __builtin_nontemporal_load((const v4u*)p); }
__device__ __forceinline__ v4u nosent(f32x4 v) {
    v4u u = __builtin_bit_cast(v4u, v);
    u.x = min(u.x, SENT - 1u); u.y = min(u.y, SENT - 1u); u.z = min(u.z, SENT - 1u); u.w = min(u.w, SENT - 1u);
    return u;
}
__device__ __forceinline__ unsigned max4(v4u a) { return max(max(a.x, a.y), max(a.z, a.w)); }

#define POLL_CHECK(b0, b1, b2, b3, b4, b5, b6, b7) \
    "v_max3_u32 v144, v" #b0 ", v" #b1 ", v" #b2 "\n\tv_max3_u32 v145, v" #b3 ", v" #b4 ", v" #b5 "\n\t" \
    "v_max3_u32 v144, v144, v" #b6 ", v" #b7 "\n\tv_max_u32 v144, v144, v145\n\tv_cmp_eq_u32 vcc, -1, v144\n\t" \
    "v_mul_f32 %0, %11, v" #b0 "\n\tv_mul_f32 %1, %11, v" #b1 "\n\tv_mul_f32 %2, %11, v" #b2 "\n\tv_mul_f32 %3, %11, v" #b3 "\n\t" \
    "v_fmac_f32 %0, %12, v" #b4 "\n\tv_fmac_f32 %1, %12, v" #b5 "\n\tv_fmac_f32 %2, %12, v" #b6 "\n\tv_fmac_f32 %3, %12, v" #b7 "\n\t" \
    "s_mov_b64 exec, vcc\n\t" \
    "s_cbranch_vccz 2f\n\t"
#define POLL_PAIR(D, r, k0, k1, k2, k3, off, srd, e0, e1) do { unsigned cnt_; u64 ex_; \
    asm volatile( \
        "s_mov_b64 %9, exec\n\t" \
        "buffer_load_dwordx4 v[128:131], %10, %13, 0 offen sc1\n\t" \
        "buffer_load_dwordx4 v[132:135], %10, %13, 0 offen offset:" #D " sc1\n\t" \
        "s_mov_b32 %4, 0x40000\n\t" \
        "s_sleep 8\n" \
        "1:\n\t" \
        "buffer_load_dwordx4 v[136:139], %10, %13, 0 offen sc1\n\t" \
        "buffer_load_dwordx4 v[140:143], %10, %13, 0 offen offset:" #D " sc1\n\t" \
        "s_waitcnt vmcnt(2)\n\t" \
        POLL_CHECK(128, 129, 130, 131, 132, 133, 134, 135) \
        "buffer_load_dwordx4 v[128:131], %10, %13, 0 offen sc1\n\t" \
        "buffer_load_dwordx4 v[132:135], %10, %13, 0 offen offset:" #D " sc1\n\t" \
        "s_waitcnt vmcnt(2)\n\t" \
        POLL_CHECK(136, 137, 138, 139, 140, 141, 142, 143) \
        "s_sub_u32 %4, %4, 1\n\t" \
        "s_cbranch_scc0 1b\n" \
        "2:\n\t" \
        "s_mov_b64 exec, %9\n\t" \
        "s_nop 4" \
        : "=&v"(r[0]), "=&v"(r[1]), "=&v"(r[2]), "=&v"(r[3]), "=&s"(cnt_), \
          "=&{v[128:131]}"(k0), "=&{v[132:135]}"(k1), "=&{v[136:139]}"(k2), "=&{v[140:143]}"(k3), "=&s"(ex_) \
        : "v"(off), "v"(e0), "v"(e1), "s"(srd) \
        : "v144", "v145", "vcc", "scc", "memory"); } while (0)
#define POLL_DRAIN(k0, k1, k2, k3) \
    asm volatile("s_waitcnt vmcnt(0)" :: "{v[128:131]}"(k0), "{v[132:135]}"(k1), "{v[136:139]}"(k2), "{v[140:143]}"(k3) : "memory")
#define SB() __builtin_amdgcn_sched_barrier(0)
#define CFENCE() asm volatile("" ::: "memory")
#define LGKM0() asm volatile("s_waitcnt lgkmcnt(0)" ::: "memory")
#define BAR() do { asm volatile("s_waitcnt lgkmcnt(0)" ::: "memory"); __builtin_amdgcn_s_barrier(); asm volatile("" ::: "memory"); } while (0)
#define LDS_WAIT(word, target, slp) \
    for (unsigned spins_ = 0; __hip_atomic_load(&(word), RLX_WG) < (target) && spins_ < (SPIN_CAP << 4); ++spins_) __builtin_amdgcn_s_sleep(slp)
#define LD_SC1(off) __builtin_amdgcn_raw_buffer_load_b128(rs, (off), 0, AUX_SC1)
#define ST_SC1(v, off) __builtin_amdgcn_raw_buffer_store_b128((v), rs, (off), 0, AUX_SC1)

__global__ void __launch_bounds__(576, 3)
fused_decode(const u16* __restrict__ x, const u16* __restrict__ Kc, const u16* __restrict__ Vc, const u16* __restrict__ wq,
             const u16* __restrict__ wo, const u16* __restrict__ w1, const u16* __restrict__ b1, const u16* __restrict__ w2,
             const u16* __restrict__ b2, u16* __restrict__ out, unsigned char* __restrict__ ws) {
    __shared__ __attribute__((aligned(16))) float s_ored[8 * 128 * 4];
    __shared__ __attribute__((aligned(16))) float s_qred[8 * 128];
    __shared__ __attribute__((aligned(16))) float s_hred[8 * 128];
    __shared__ __attribute__((aligned(16))) unsigned s_q[256];
    __shared__ __attribute__((aligned(16))) u16 s_xn[2 * 512];
    __shared__ __attribute__((aligned(16))) unsigned s_h[16];
    __shared__ __attribute__((aligned(16))) unsigned s_o[32];
    __shared__ __attribute__((aligned(16))) float s_ml[8 * 4 * 2];
    __shared__ __attribute__((aligned(16))) float s_fw[4 * 8];
    __shared__ __attribute__((aligned(16))) float s_fin[8 * 32];
    __shared__ unsigned s_flag, s_cnt;

    asm volatile("" :: "s"(x), "s"(Kc), "s"(Vc), "s"(wq), "s"(wo), "s"(w1), "s"(b1), "s"(w2), "s"(b2), "s"(out), "s"(ws));
    const int L = blockIdx.x;
    const int b = L >> 7, kvh = (L >> 4) & 7, s = L & 15, grp = L >> 4, c2 = L & 127;
    const int qline = (s + kvh) & 7;
    const int mi = 2 * b + (s >> 3);
    const int lane = threadIdx.x & 63, w0 = __builtin_amdgcn_readfirstlane(threadIdx.x >> 6);
    const int w = w0 - 1, tid = w * 64 + lane;
    const int rho = lane >> 4, j2 = lane & 7;
    const unsigned r16 = lane & 15;
    const __amdgpu_buffer_rsrc_t rs = __builtin_amdgcn_make_buffer_rsrc(ws, 0, WS_BYTES, 0x00020000);
    const v4u srd = {(unsigned)(uintptr_t)ws, (unsigned)((uintptr_t)ws >> 32) & 0xffffu, (unsigned)WS_BYTES, 0x00020000u};
    const bool m1_on = L < 224, m2_on = c2 < 112;
    const int mj = m1_on ? L % 28 : L - 224, mks = m1_on ? L / 28 : 0;
    const int hblk = m2_on ? c2 : c2 - 16;
    const bool fin_on = (s & 7) == kvh;
    const int fj = 2 * kvh + (s >> 3);

    if (w0 == 0) {
        __builtin_amdgcn_s_setprio(3);
        const v4u xa0 = *(const v4u*)(x + 16 * lane), xa1 = *(const v4u*)(x + 16 * lane + 8);
        CFENCE();
        const u64 epoch = __hip_atomic_load((GAS u64*)(ws + WS_EPOCH), RLX_AGENT);
        if (lane == 0) { s_flag = 0; s_cnt = 0; }
        BAR();
        const unsigned b1v = *(const unsigned*)(b1 + hblk * 16 + 2 * j2);
        const int ocol = 32 * fj + (lane & 31);
        const unsigned xo = x[b * 512 + ocol], b2o = b2[ocol];
        CFENCE();
        {
            const v4u sv = {SENT, SENT, SENT, SENT};
            const int base = WS_BLK + L * BLK_SZ + lane * 16, base2 = WS_IN2 + L * 2048 + lane * 16;
            ST_SC1(sv, base);
            ST_SC1(sv, base + 1024);
            ST_SC1(sv, lane < 8 ? base + 2048 : 0x7ffffff0);
            ST_SC1(sv, base2);
            ST_SC1(sv, base2 + 1024);
        }
        {
            float s1 = 0.f, s2 = 0.f, t1 = 0.f, t2 = 0.f;
#pragma unroll
            for (int k = 0; k < 4; ++k) {
                s1 = dot2(xa0[k], 0x3f803f80u, s1); t1 = dot2(xa1[k], 0x3f803f80u, t1);
                s2 = dot2(xa0[k], xa0[k], s2); t2 = dot2(xa1[k], xa1[k], t2);
            }
            s1 = row_sum(s1 + t1); s2 = row_sum(s2 + t2);
            s1 = sw16_add(s1, s1); s2 = sw16_add(s2, s2);
            const float mean = s1 * (1.0f / 512.0f);
            const float var = fmaxf(fmaf(-mean, mean, s2 * (1.0f / 512.0f)), 0.f);
            const float rstd = __builtin_amdgcn_rsqf(var + 1e-5f), nb = -mean * rstd;
            v4u o0, o1;
#pragma unroll
            for (int k = 0; k < 4; ++k) {
                o0[k] = pk_bf16(fmaf(bflo(xa0[k]), rstd, nb), fmaf(bfhi(xa0[k]), rstd, nb));
                o1[k] = pk_bf16(fmaf(bflo(xa1[k]), rstd, nb), fmaf(bfhi(xa1[k]), rstd, nb));
            }
            *(v4u*)&s_xn[16 * lane] = o0;
            *(v4u*)&s_xn[16 * lane + 8] = o1;
        }
        BAR();
        const u64 tag = epoch + 1ull;
        const unsigned tg0 = (unsigned)tag, tg1 = (unsigned)(tag >> 32), thi8 = (unsigned)(tag >> 8), tlo8 = tg0 << 24;
        asm volatile("" :: "v"(thi8), "v"(tlo8));
        BAR();
        {
            const int c = lane & 31, pos = (lane >> 5) * 64 + ((c >> 1) & 1) * 32 + (c & 1) * 16 + (c >> 2);
            float q0 = 0.f, q1 = 0.f, h0 = 0.f, h1 = 0.f;
#pragma unroll
            for (int k = 0; k < 8; ++k) { q0 += s_qred[k * 128 + pos]; q1 += s_qred[k * 128 + pos + 8]; }
            ST_SC1((v4u{tlo8 | (__float_as_uint(q0) >> 8), thi8, tlo8 | (__float_as_uint(q1) >> 8), thi8}),
                   WS_QA + (kvh * 8 + qline) * 4096 + rho * 1024 + mi * 256 + r16 * 16);
            CFENCE();
#pragma unroll
            for (int k = 0; k < 8; ++k) { h0 += s_hred[k * 128 + pos]; h1 += s_hred[k * 128 + pos + 8]; }
            if (m1_on)
                ST_SC1((v4u{tlo8 | (__float_as_uint(h0) >> 8), thi8, tlo8 | (__float_as_uint(h1) >> 8), thi8}),
                       WS_HG + ((lane >> 5) * 128 + 4 * mj + (c >> 3)) * 1024 + mks * 128 + (c & 7) * 16);
        }
        __builtin_amdgcn_s_sleep(48);
        {
            v4u ga;
            for (unsigned spins = 0;; ++spins) {
                CFENCE();
                ga = LD_SC1(WS_QA + (kvh * 8 + qline) * 4096 + mi * 1024 + lane * 16);
                const unsigned bad = (ga.y ^ thi8) | (ga.w ^ thi8) | (((ga.x ^ tlo8) | (ga.z ^ tlo8)) >> 24);
                if (__all(bad == 0u) || spins > SPIN_CAP) break;
                __builtin_amdgcn_s_sleep(4);
            }
            asm volatile("s_waitcnt vmcnt(0)" ::: "memory");
            float qa = __uint_as_float(ga.x << 8), qb = __uint_as_float(ga.z << 8);
            qa = sw16_add(qa, qa); qb = sw16_add(qb, qb);
            qa = sw32_add(qa, qa); qb = sw32_add(qb, qb);
            const unsigned pk = pk_bf16(qa, qb), pk2 = __float_as_uint(dpp<XOR1>(__uint_as_float(pk)));
            if (lane < 16 && !(lane & 1)) ST_SC1((v4u{pk, tg0, pk2, tg0}), WS_QG + grp * 2048 + (2 * qline + (s >> 3)) * 128 + lane * 8);
            if (lane == 16) __hip_atomic_store((GAS u64*)(ws + WS_FLAGS) + L, tag, RLX_AGENT);
        }
        __builtin_amdgcn_s_sleep(56);
        {
            v4u g[2], gh, gf;
            unsigned bad[2] = {1u, 1u}, badh = 1u, badf = 1u;
            bool qdone = false;
            for (unsigned spins = 0;; ++spins) {
                CFENCE();
#pragma unroll
                for (int k = 0; k < 2; ++k) if (bad[k]) g[k] = LD_SC1(WS_QG + grp * 2048 + k * 1024 + lane * 16);
                if (badh) gh = LD_SC1(WS_HG + (b * 128 + hblk) * 1024 + lane * 16);
                if (badf) gf = LD_SC1(WS_FLAGS + b * 1024 + lane * 16);
#pragma unroll
                for (int k = 0; k < 2; ++k) bad[k] = (g[k].y ^ tg0) | (g[k].w ^ tg0);
                if (!qdone && (__all((bad[0] | bad[1]) == 0u) || spins > SPIN_CAP)) {
                    qdone = true;
                    *(v2u*)&s_q[2 * lane] = v2u{g[0].x, g[0].z};
                    *(v2u*)&s_q[128 + 2 * lane] = v2u{g[1].x, g[1].z};
                    LGKM0();
                    if (lane == 0) __hip_atomic_store(&s_flag, 1u, RLX_WG);
                }
                badh = (gh.y ^ thi8) | (gh.w ^ thi8) | (((gh.x ^ tlo8) | (gh.z ^ tlo8)) >> 24);
                badf = (gf.x ^ tg0) | (gf.y ^ tg1) | (gf.z ^ tg0) | (gf.w ^ tg1);
                if ((qdone && __all((badh | badf) == 0u)) || spins > SPIN_CAP) break;
                __builtin_amdgcn_s_sleep(4);
            }
            if (lane == 0) __hip_atomic_store(&s_flag, 2u, RLX_WG);
            float hv[2] = {__uint_as_float(gh.x << 8), __uint_as_float(gh.z << 8)};
#pragma unroll
            for (int k = 0; k < 2; ++k) {
                float h = hv[k];
                h += dpp<ROR8>(h); h = sw16_add(h, h); h = sw32_add(h, h);
                h += k ? bfhi(b1v) : bflo(b1v);
                const float u2 = 1.5957691216057308f * (h + 0.044715f * h * h * h);
                const float th = 1.0f - 2.0f / (__expf(u2) + 1.0f);
                hv[k] = m2_on ? 0.5f * h * (1.0f + th) : 0.f;
            }
            if (lane < 8) *(v2u*)&s_h[2 * lane] = v2u{pk_bf16(hv[0], hv[0]), pk_bf16(hv[1], hv[1])};
        }
        LDS_WAIT(s_cnt, 8u, 2);
        CFENCE();
        {
            const int h = (lane >> 3) & 3;
            const f32x2 ml = *(const f32x2*)&s_ml[(j2 * 4 + h) * 2];
            const float M = oct_max(ml.x);
            const float f = __builtin_amdgcn_exp2f(ml.x - M);
            const float Ls = oct_sum(f * ml.y);
            if (lane < 32) s_fw[lane] = f;
            if (lane < 32 && j2 < 4) {
                const v2u mlv = {min(__float_as_uint(M), SENT - 1u), min(__float_as_uint(Ls), SENT - 1u)};
                __builtin_amdgcn_raw_buffer_store_b64(mlv, rs, WS_BLK + (grp * 16 + 4 * h + j2) * BLK_SZ + BLK_ML + s * 8, 0, AUX_SC1);
            }
        }
        BAR();
        v4u k0, k1, k2, k3;
        {
            const int base = WS_BLK + L * BLK_SZ;
            const int oa = base + BLK_IN1 + (2 * j2) * 128 + (lane >> 3) * 16;
            v4u ML;
            for (unsigned spins = 0;; ++spins) {
                CFENCE();
                ML = LD_SC1(base + BLK_ML + j2 * 16);
                if (__all(max4(ML) != SENT) || spins > SPIN_CAP) break;
                __builtin_amdgcn_s_sleep(2);
            }
            const float M0 = __uint_as_float(ML.x), L0 = __uint_as_float(ML.y), M1 = __uint_as_float(ML.z), L1 = __uint_as_float(ML.w);
            const float Mx = oct_max(fmaxf(M0, M1));
            float e0 = __builtin_amdgcn_exp2f(M0 - Mx), e1 = __builtin_amdgcn_exp2f(M1 - Mx);
            const float inv = __builtin_amdgcn_rcpf(oct_sum(fmaf(e0, L0, e1 * L1)));
            e0 *= inv; e1 *= inv;
            float o4[4];
            POLL_PAIR(128, o4, k0, k1, k2, k3, oa, srd, e0, e1);
            v4u ob;
#pragma unroll
            for (int t = 0; t < 4; ++t) { const float o = oct_sum(o4[t]); ob[t] = pk_bf16(o, o); }
            if (j2 == 0) *(v4u*)&s_o[(lane >> 3) * 4] = ob;
        }
        BAR();
        POLL_DRAIN(k0, k1, k2, k3);
        if (fin_on) {
            BAR();
            float r = bflo(xo) + bflo(b2o);
#pragma unroll
            for (int k = 0; k < 8; ++k) r += s_fin[k * 32 + (lane & 31)];
            if (lane < 32) out[b * 512 + ocol] = (u16)(pk_bf16(r, r) & 0xffffu);
            if (L == 0 && lane == 0) __hip_atomic_store((GAS u64*)(ws + WS_EPOCH), tag, RLX_AGENT);
        }
        return;
    }

    v4u wqr[2], w1r;
    {
        const u16* p = wq + (size_t)(128 * mi + 16 * w) * 4096 + kvh * 512 + qline * 64;
        const unsigned lo = (unsigned)(lane >> 3) * 4096u + 8u * j2;
        wqr[0] = *(const v4u*)(p + lo);
        wqr[1] = *(const v4u*)(p + lo + 8 * 4096);
        CFENCE();
        w1r = *(const v4u*)(w1 + (size_t)(64 * mks + 8 * w) * 1792 + mj * 64 + ((unsigned)(lane >> 3) * 1792u + 8u * j2));
    }
    CFENCE(); SB();
    BAR();
    const unsigned char* kb = (const unsigned char*)Kc + (size_t)grp * 8192 * 256;
    const unsigned char* vb = (const unsigned char*)Vc + (size_t)grp * 8192 * 256;
    const unsigned klo = (4u * (r16 & 3) + ((r16 >> 2) & 1)) * 256u + 64u * (r16 >> 3) + 16u * rho;
    const unsigned vlo = 16u * lane;
#define KIN(i) ((((i) >> 1) & 1) * 512 + ((i) & 1) * 128)
#define TOFF(u) ((unsigned)(((u) * 128 + s * 8 + w + 32 * grp) & 511) * 4096u)
    v4u kr[16], vr[16];
#pragma unroll
    for (int i = 0; i < K0; ++i) kr[i] = ld_nt(kb + TOFF(i >> 2) + klo + KIN(i));
    CFENCE(); SB();
    const f32x4 z4 = {0.f, 0.f, 0.f, 0.f};
    BAR();
    {
        const int xi = ((lane >> 2) & 1) * 512 + 2 * rho + ((lane >> 3) & 1);
        const unsigned sh = (lane & 1) * 16;
        const unsigned e0 = (unsigned)s_xn[xi + 128 * mi + 16 * w] << sh, e1 = (unsigned)s_xn[xi + 128 * mi + 16 * w + 8] << sh;
        const unsigned e2 = (unsigned)s_xn[xi + 64 * mks + 8 * w] << sh;
        const bool lo2 = (lane & 2) == 0;
        const unsigned a0 = lo2 ? e0 : 0u, c0 = lo2 ? 0u : e0, a1 = lo2 ? e1 : 0u, c1 = lo2 ? 0u : e1, a2 = lo2 ? e2 : 0u, c2_ = lo2 ? 0u : e2;
        const f32x4 d0 = mfma16(wqr[1].x, wqr[1].y, a1, c1, mfma16(wqr[0].x, wqr[0].y, a0, c0, z4));
        const f32x4 d1 = mfma16(wqr[1].z, wqr[1].w, a1, c1, mfma16(wqr[0].z, wqr[0].w, a0, c0, z4));
        f32x4 u;
#pragma unroll
        for (int i = 0; i < 4; ++i) {
            auto r = __builtin_amdgcn_permlane32_swap(__float_as_uint(d0[i]), __float_as_uint(d1[i]), false, false);
            u[i] = __uint_as_float(r[0]) + dpp<ROR8>(__uint_as_float(r[1]));
        }
        const int pos = w * 128 + ((lane >> 2) & 1) * 64 + (rho >> 1) * 32 + (lane & 3) * 8 + (rho & 1) * 4;
        if ((lane & 8) == 0) *(f32x4*)&s_qred[pos] = u;
        const f32x4 g0 = mfma16(w1r.x, w1r.y, a2, c2_, z4), g1 = mfma16(w1r.z, w1r.w, a2, c2_, z4);
#pragma unroll
        for (int i = 0; i < 4; ++i) {
            auto r = __builtin_amdgcn_permlane32_swap(__float_as_uint(g0[i]), __float_as_uint(g1[i]), false, false);
            u[i] = __uint_as_float(r[0]) + dpp<ROR8>(__uint_as_float(r[1]));
        }
        if ((lane & 8) == 0) *(f32x4*)&s_hred[pos] = u;
    }
    BAR();
    unsigned qrdy = 0;
#define WAITW() do { if (!qrdy) { qrdy = __builtin_amdgcn_readfirstlane(__hip_atomic_load(&s_flag, RLX_WG)) >> 1; \
        if (!qrdy) asm volatile("s_waitcnt vmcnt(2)" ::: "memory"); } } while (0)
#pragma unroll
    for (int i = K0; i < 16; ++i) { WAITW(); kr[i] = ld_nt(kb + TOFF(i >> 2) + klo + KIN(i)); CFENCE(); }
#pragma unroll
    for (int i = 0; i < 16; ++i) { WAITW(); vr[i] = ld_nt(vb + TOFF(i >> 2) + vlo + (i & 3) * 1024); CFENCE(); }
    if (!qrdy) LDS_WAIT(s_flag, 1u, 1);
    CFENCE(); SB();
    constexpr float C2 = 0.08838834764831845f * 1.4426950408889634f;
    unsigned pp[4][4];
    {
        f32x4 S[4];
        v4u qf[2];
#pragma unroll
        for (int hf = 0; hf < 2; ++hf) qf[hf] = *(const v4u*)&s_q[(r16 >> 2) * 64 + 32 * hf + 16 * ((r16 >> 1) & 1) + 4 * rho];
#pragma unroll
        for (int u = 0; u < 4; ++u) {
            const f32x4 da = mfma32(kr[u * 4 + 1], qf[1], mfma32(kr[u * 4 + 0], qf[0], z4));
            const f32x4 db = mfma32(kr[u * 4 + 3], qf[1], mfma32(kr[u * 4 + 2], qf[0], z4));
#pragma unroll
            for (int i = 0; i < 4; ++i) {
                auto r = __builtin_amdgcn_permlane32_swap(__float_as_uint(da[i]), __float_as_uint(db[i]), false, false);
                S[u][i] = dpp<BC0>(__uint_as_float(r[0])) + dpp<BC2>(__uint_as_float(r[1]));
            }
        }
        float m = S[0][0];
#pragma unroll
        for (int u = 0; u < 4; ++u)
#pragma unroll
            for (int i = 0; i < 4; ++i) m = fmaxf(m, S[u][i]);
        m = sw16_max(m); m = sw32_max(m);
        m *= C2;
        float l4[4] = {0.f, 0.f, 0.f, 0.f};
#pragma unroll
        for (int u = 0; u < 4; ++u)
#pragma unroll
            for (int i = 0; i < 4; ++i) { const float p = __builtin_amdgcn_exp2f(fmaf(S[u][i], C2, -m)); l4[i] += p; pp[u][i] = pk_bf16(p, p); }
        float l = (l4[0] + l4[1]) + (l4[2] + l4[3]);
        l = sw16_add(l, l); l = sw32_add(l, l);
        if (lane < 16 && (lane & 3) == 0) *(f32x2*)&s_ml[(w * 4 + (lane >> 2)) * 2] = f32x2{m, l};
    }
    LGKM0();
    if (lane == 0) __hip_atomic_fetch_add(&s_cnt, 1u, RLX_WG);
    {
        const unsigned hm = (lane & 1) ? 0xffff0000u : 0x0000ffffu;
        const unsigned ma = (lane & 2) ? 0u : hm, mb = (lane & 2) ? hm : 0u;
        f32x4 O0 = z4, O1 = z4;
#pragma unroll
        for (int u = 0; u < 4; ++u)
#pragma unroll
            for (int i = 0; i < 4; ++i) {
                const unsigned pa = pp[u][i] & ma, pb = pp[u][i] & mb;
                const v4u v = vr[u * 4 + i];
                O0 = mfma16(v.x, v.y, pa, pb, O0);
                O1 = mfma16(v.z, v.w, pa, pb, O1);
            }
        float* po = &s_ored[(w * 128 + (r16 >> 2) * 32 + rho * 8 + (lane & 3)) * 4];
        *(f32x4*)po = O0;
        *(f32x4*)(po + 16) = O1;
    }
    int o_pub1 = WS_BLK + (grp * 16 + (tid >> 3)) * BLK_SZ + BLK_IN1 + s * 128 + j2 * 16;
    int o_pub2 = WS_IN2 + (b * 16 + 2 * w + rho) * 16384 + c2 * 128 + j2 * 16;
    int o_fin = WS_IN2 + (b * 16 + fj) * 16384 + w * 2048 + lane * 16;
    asm volatile("" : "+v"(o_pub1), "+v"(o_pub2), "+v"(o_fin));
    CFENCE(); SB();
    BAR();
    if (w < 2) {
        const f32x4 fa = *(const f32x4*)&s_fw[(tid >> 5) * 8], fb = *(const f32x4*)&s_fw[(tid >> 5) * 8 + 4];
        f32x4 acc = fa[0] * *(const f32x4*)&s_ored[tid * 4];
#pragma unroll
        for (int k = 1; k < 8; ++k) acc += (k < 4 ? fa[k & 3] : fb[k & 3]) * *(const f32x4*)&s_ored[(k * 128 + tid) * 4];
        ST_SC1(nosent(acc), o_pub1);
    }
    CFENCE();
    v4u wor[4], w2r[2];
    {
        const u16* p = wo + (size_t)(kvh * 512 + s * 32) * 512 + 64 * w;
        const u16* p2 = w2 + (size_t)hblk * 16 * 512 + 64 * w;
        const unsigned lo = (unsigned)(lane >> 3) * 512u + 8u * j2;
        w2r[0] = *(const v4u*)(p2 + lo); w2r[1] = *(const v4u*)(p2 + lo + 8 * 512);
#pragma unroll
        for (int t = 0; t < 4; ++t) wor[t] = *(const v4u*)(p + lo + t * 8 * 512);
    }
    CFENCE(); SB();
    unsigned msk[4];
#pragma unroll
    for (int k = 0; k < 4; ++k) msk[k] = (k == (j2 >> 1)) ? ((j2 & 1) ? 0xffff0000u : 0x0000ffffu) : 0u;
    const unsigned ph0 = s_h[lane >> 3], ph1 = s_h[(lane >> 3) + 8];
    v4u bh0 = {ph0 & msk[0], ph0 & msk[1], ph0 & msk[2], ph0 & msk[3]}, bh1 = {ph1 & msk[0], ph1 & msk[1], ph1 & msk[2], ph1 & msk[3]};
    asm volatile("" : "+v"(bh0), "+v"(bh1));
    BAR();
    {
        const v4u ob = *(const v4u*)&s_o[(lane >> 3) * 4];
        v4u bo[4];
#pragma unroll
        for (int t = 0; t < 4; ++t) bo[t] = v4u{ob[t] & msk[0], ob[t] & msk[1], ob[t] & msk[2], ob[t] & msk[3]};
        asm volatile("" : "+v"(bo[0]), "+v"(bo[1]), "+v"(bo[2]), "+v"(bo[3]));
        f32x4 Y = mfma32(w2r[1], bh1, mfma32(w2r[0], bh0, z4));
#pragma unroll
        for (int t = 0; t < 4; ++t) Y = mfma32(wor[t], bo[t], Y);
        f32x4 Z;
#pragma unroll
        for (int i = 0; i < 4; ++i) {
            const float y = dpp<ROR8>(Y[i]);
            auto r = __builtin_amdgcn_permlane32_swap(__float_as_uint(y), __float_as_uint(y), false, false);
            Z[i] = Y[i] + __uint_as_float(r[1]);
        }
        if (lane < 32 && (lane & 8) == 0) ST_SC1(nosent(Z), o_pub2);
    }
    if (fin_on) {
        v4u k0, k1, k2, k3;
        float v[4];
        const float one = 1.0f;
        __builtin_amdgcn_s_sleep(10);
        POLL_PAIR(1024, v, k0, k1, k2, k3, o_fin, srd, one, one);
        const float t0 = sw32_add(v[0], v[1]), t1 = sw32_add(v[2], v[3]);
        float r = sw16_add(t0, t1);
        r += dpp<ROR8>(r);
        if ((lane & 8) == 0) s_fin[w * 32 + 8 * (2 * (rho & 1) + (rho >> 1)) + j2] = r;
        BAR();
        POLL_DRAIN(k0, k1, k2, k3);
    }
}

extern "C" void kernel_launch(void* const* d_in, const int* in_sizes, int n_in, void* d_out, int out_size, void* d_ws, size_t ws_size,
                              hipStream_t stream) {
    fused_decode<<<256, 576, 0, stream>>>((const u16*)d_in[0], (const u16*)d_in[1], (const u16*)d_in[2], (const u16*)d_in[3], (const u16*)d_in[4],
                                          (const u16*)d_in[6], (const u16*)d_in[7], (const u16*)d_in[8], (const u16*)d_in[9], (u16*)d_out,
                                          (unsigned char*)d_ws);
}
```

```cpp
#include <hip/hip_runtime.h>
#include <stdint.h>

typedef unsigned short u16;
typedef unsigned long long u64;
typedef unsigned v4u __attribute__((ext_vector_type(4)));
typedef unsigned v2u __attribute__((ext_vector_type(2)));
typedef float f32x4 __attribute__((ext_vector_type(4)));
typedef float f32x2 __attribute__((ext_vector_type(2)));
typedef short s16x4 __attribute__((ext_vector_type(4)));
typedef __bf16 bf16x8 __attribute__((ext_vector_type(8)));
typedef __bf16 bf16x2 __attribute__((ext_vector_type(2)));
#define GAS __attribute__((address_space(1)))
#define RLX_AGENT __ATOMIC_RELAXED, __HIP_MEMORY_SCOPE_AGENT
#define RLX_WG __ATOMIC_RELAXED, __HIP_MEMORY_SCOPE_WORKGROUP

constexpr int K0 = 5;
constexpr int WS_EPOCH = 0;
constexpr int WS_FLAGS = 4096;
constexpr int WS_QG = 8192;
constexpr int WS_BLK = 131072;
constexpr int BLK_SZ = 3072, BLK_IN1 = 0, BLK_ML = 2048;
constexpr int WS_IN2 = WS_BLK + 256 * BLK_SZ;
constexpr int WS_QA = WS_IN2 + 32 * 16384;
constexpr int WS_HG = WS_QA + 64 * 4096;
constexpr int WS_BYTES = WS_HG + 2 * 128 * 1024;
constexpr unsigned SENT = 0xFFFFFFFFu;
constexpr int AUX_SC1 = 16;
constexpr unsigned SPIN_CAP = 1u << 18;

template <int CTRL> __device__ __forceinline__ float dpp(float x) {
    return __builtin_bit_cast(float, __builtin_amdgcn_mov_dpp(__builtin_bit_cast(int, x), CTRL, 0xf, 0xf, true));
}
constexpr int XOR1 = 0xB1, XOR2 = 0x4E, HMIR = 0x141, ROR8 = 0x128, BC0 = 0x00, BC2 = 0xAA;
__device__ __forceinline__ float sw32_add(float a, float b) {
    auto r = __builtin_amdgcn_permlane32_swap(__float_as_uint(a), __float_as_uint(b), false, false);
    return __uint_as_float(r[0]) + __uint_as_float(r[1]);
}
__device__ __forceinline__ float sw16_add(float a, float b) {
    auto r = __builtin_amdgcn_permlane16_swap(__float_as_uint(a), __float_as_uint(b), false, false);
    return __uint_as_float(r[0]) + __uint_as_float(r[1]);
}
__device__ __forceinline__ float sw32_max(float a) {
    auto r = __builtin_amdgcn_permlane32_swap(__float_as_uint(a), __float_as_uint(a), false, false);
    return fmaxf(__uint_as_float(r[0]), __uint_as_float(r[1]));
}
__device__ __forceinline__ float sw16_max(float a) {
    auto r = __builtin_amdgcn_permlane16_swap(__float_as_uint(a), __float_as_uint(a), false, false);
    return fmaxf(__uint_as_float(r[0]), __uint_as_float(r[1]));
}
__device__ __forceinline__ float oct_sum(float x) { x += dpp<XOR1>(x); x += dpp<XOR2>(x); x += dpp<HMIR>(x); return x; }
__device__ __forceinline__ float oct_max(float x) { x = fmaxf(x, dpp<XOR1>(x)); x = fmaxf(x, dpp<XOR2>(x)); x = fmaxf(x, dpp<HMIR>(x)); return x; }
__device__ __forceinline__ float row_sum(float x) { x = oct_sum(x); x += dpp<ROR8>(x); return x; }
__device__ __forceinline__ float bflo(unsigned u) { return __uint_as_float(u << 16); }
__device__ __forceinline__ float bfhi(unsigned u) { return __uint_as_float(u & 0xffff0000u); }
__device__ __forceinline__ unsigned pk_bf16(float lo, float hi) {
    f32x2 v = {lo, hi};
    return __builtin_bit_cast(unsigned, __builtin_convertvector(v, bf16x2));
}
__device__ __forceinline__ float dot2(unsigned a, unsigned b, float c) {
    return __builtin_amdgcn_fdot2_f32_bf16(__builtin_bit_cast(bf16x2, a), __builtin_bit_cast(bf16x2, b), c, false);
}
__device__ __forceinline__ f32x4 mfma32(v4u a, v4u b, f32x4 c) {
    return __builtin_amdgcn_mfma_f32_16x16x32_bf16(__builtin_bit_cast(bf16x8, a), __builtin_bit_cast(bf16x8, b), c, 0, 0, 0);
}
__device__ __forceinline__ f32x4 mfma16(unsigned a0, unsigned a1, unsigned b0, unsigned b1, f32x4 c) {
    return __builtin_amdgcn_mfma_f32_16x16x16bf16_1k(__builtin_bit_cast(s16x4, v2u{a0, a1}), __builtin_bit_cast(s16x4, v2u{b0, b1}), c, 0, 0, 0);
}
__device__ __forceinline__ v4u ld_nt(const void* p) { return __builtin_nontemporal_load((const v4u*)p); }
__device__ __forceinline__ v4u nosent(f32x4 v) {
    v4u u = __builtin_bit_cast(v4u, v);
    u.x = min(u.x, SENT - 1u); u.y = min(u.y, SENT - 1u); u.z = min(u.z, SENT - 1u); u.w = min(u.w, SENT - 1u);
    return u;
}
__device__ __forceinline__ unsigned max4(v4u a) { return max(max(a.x, a.y), max(a.z, a.w)); }

#define POLL_CHECK(b0, b1, b2, b3, b4, b5, b6, b7) \
    "v_max3_u32 v144, v" #b0 ", v" #b1 ", v" #b2 "\n\tv_max3_u32 v145, v" #b3 ", v" #b4 ", v" #b5 "\n\t" \
    "v_max3_u32 v144, v144, v" #b6 ", v" #b7 "\n\tv_max_u32 v144, v144, v145\n\tv_cmp_eq_u32 vcc, -1, v144\n\t" \
    "v_mul_f32 %0, %11, v" #b0 "\n\tv_mul_f32 %1, %11, v" #b1 "\n\tv_mul_f32 %2, %11, v" #b2 "\n\tv_mul_f32 %3, %11, v" #b3 "\n\t" \
    "v_fmac_f32 %0, %12, v" #b4 "\n\tv_fmac_f32 %1, %12, v" #b5 "\n\tv_fmac_f32 %2, %12, v" #b6 "\n\tv_fmac_f32 %3, %12, v" #b7 "\n\t" \
    "s_mov_b64 exec, vcc\n\t" \
    "s_cbranch_vccz 2f\n\t"
#define POLL_PAIR(D, r, k0, k1, k2, k3, off, srd, e0, e1) do { unsigned cnt_; u64 ex_; \
    asm volatile( \
        "s_mov_b64 %9, exec\n\t" \
        "buffer_load_dwordx4 v[128:131], %10, %13, 0 offen sc1\n\t" \
        "buffer_load_dwordx4 v[132:135], %10, %13, 0 offen offset:" #D " sc1\n\t" \
        "s_mov_b32 %4, 0x40000\n\t" \
        "s_sleep 8\n" \
        "1:\n\t" \
        "buffer_load_dwordx4 v[136:139], %10, %13, 0 offen sc1\n\t" \
        "buffer_load_dwordx4 v[140:143], %10, %13, 0 offen offset:" #D " sc1\n\t" \
        "s_waitcnt vmcnt(2)\n\t" \
        POLL_CHECK(128, 129, 130, 131, 132, 133, 134, 135) \
        "buffer_load_dwordx4 v[128:131], %10, %13, 0 offen sc1\n\t" \
        "buffer_load_dwordx4 v[132:135], %10, %13, 0 offen offset:" #D " sc1\n\t" \
        "s_waitcnt vmcnt(2)\n\t" \
        POLL_CHECK(136, 137, 138, 139, 140, 141, 142, 143) \
        "s_sub_u32 %4, %4, 1\n\t" \
        "s_cbranch_scc0 1b\n" \
        "2:\n\t" \
        "s_mov_b64 exec, %9\n\t" \
        "s_nop 4" \
        : "=&v"(r[0]), "=&v"(r[1]), "=&v"(r[2]), "=&v"(r[3]), "=&s"(cnt_), \
          "=&{v[128:131]}"(k0), "=&{v[132:135]}"(k1), "=&{v[136:139]}"(k2), "=&{v[140:143]}"(k3), "=&s"(ex_) \
        : "v"(off), "v"(e0), "v"(e1), "s"(srd) \
        : "v144", "v145", "vcc", "scc", "memory"); } while (0)
#define POLL_DRAIN(k0, k1, k2, k3) \
    asm volatile("s_waitcnt vmcnt(0)" :: "{v[128:131]}"(k0), "{v[132:135]}"(k1), "{v[136:139]}"(k2), "{v[140:143]}"(k3) : "memory")
#define SB() __builtin_amdgcn_sched_barrier(0)
#define CFENCE() asm volatile("" ::: "memory")
#define LGKM0() asm volatile("s_waitcnt lgkmcnt(0)" ::: "memory")
#define BAR() do { asm volatile("s_waitcnt lgkmcnt(0)" ::: "memory"); __builtin_amdgcn_s_barrier(); asm volatile("" ::: "memory"); } while (0)
#define LDS_WAIT(word, target, slp) \
    for (unsigned spins_ = 0; __hip_atomic_load(&(word), RLX_WG) < (target) && spins_ < (SPIN_CAP << 4); ++spins_) __builtin_amdgcn_s_sleep(slp)
#define LD_SC1(off) __builtin_amdgcn_raw_buffer_load_b128(rs, (off), 0, AUX_SC1)
#define ST_SC1(v, off) __builtin_amdgcn_raw_buffer_store_b128((v), rs, (off), 0, AUX_SC1)

__global__ void __launch_bounds__(576, 3)
fused_decode(const u16* __restrict__ x, const u16* __restrict__ Kc, const u16* __restrict__ Vc, const u16* __restrict__ wq,
             const u16* __restrict__ wo, const u16* __restrict__ w1, const u16* __restrict__ b1, const u16* __restrict__ w2,
             const u16* __restrict__ b2, u16* __restrict__ out, unsigned char* __restrict__ ws) {
    __shared__ __attribute__((aligned(16))) float s_ored[8 * 128 * 4];
    __shared__ __attribute__((aligned(16))) float s_qred[8 * 128];
    __shared__ __attribute__((aligned(16))) float s_hred[8 * 128];
    __shared__ __attribute__((aligned(16))) unsigned s_q[256];
    __shared__ __attribute__((aligned(16))) u16 s_xn[2 * 512];
    __shared__ __attribute__((aligned(16))) unsigned s_h[16];
    __shared__ __attribute__((aligned(16))) unsigned s_o[32];
    __shared__ __attribute__((aligned(16))) float s_ml[8 * 4 * 2];
    __shared__ __attribute__((aligned(16))) float s_fw[4 * 8];
    __shared__ __attribute__((aligned(16))) float s_fin[8 * 32];
    __shared__ unsigned s_flag, s_cnt;

    asm volatile("" :: "s"(x), "s"(Kc), "s"(Vc), "s"(wq), "s"(wo), "s"(w1), "s"(b1), "s"(w2), "s"(b2), "s"(out), "s"(ws));
    const int L = blockIdx.x;
    const int b = L >> 7, kvh = (L >> 4) & 7, s = L & 15, grp = L >> 4, c2 = L & 127;
    const int qline = (s + kvh) & 7;
    const int mi = 2 * b + (s >> 3);
    const int lane = threadIdx.x & 63, w0 = __builtin_amdgcn_readfirstlane(threadIdx.x >> 6);
    const int w = w0 - 1, tid = w * 64 + lane;
    const int rho = lane >> 4, j2 = lane & 7;
    const unsigned r16 = lane & 15;
    const __amdgpu_buffer_rsrc_t rs = __builtin_amdgcn_make_buffer_rsrc(ws, 0, WS_BYTES, 0x00020000);
    const v4u srd = {(unsigned)(uintptr_t)ws, (unsigned)((uintptr_t)ws >> 32) & 0xffffu, (unsigned)WS_BYTES, 0x00020000u};
    const bool m1_on = L < 224, m2_on = c2 < 112;
    const int mj = m1_on ? L % 28 : L - 224, mks = m1_on ? L / 28 : 0;
    const int hblk = m2_on ? c2 : c2 - 16;
    const bool fin_on = (s & 7) == kvh;
    const int fj = 2 * kvh + (s >> 3);

    if (w0 == 0) {
        __builtin_amdgcn_s_setprio(3);
        const v4u xa0 = *(const v4u*)(x + 16 * lane), xa1 = *(const v4u*)(x + 16 * lane + 8);
        CFENCE();
        const u64 epoch = __hip_atomic_load((GAS u64*)(ws + WS_EPOCH), RLX_AGENT);
        if (lane == 0) { s_flag = 0; s_cnt = 0; }
        BAR();
        const unsigned b1v = *(const unsigned*)(b1 + hblk * 16 + 2 * j2);
        const int ocol = 32 * fj + (lane & 31);
        const unsigned xo = x[b * 512 + ocol], b2o = b2[ocol];
        CFENCE();
        {
            const v4u sv = {SENT, SENT, SENT, SENT};
            const int base = WS_BLK + L * BLK_SZ + lane * 16, base2 = WS_IN2 + L * 2048 + lane * 16;
            ST_SC1(sv, base);
            ST_SC1(sv, base + 1024);
            ST_SC1(sv, lane < 8 ? base + 2048 : 0x7ffffff0);
            ST_SC1(sv, base2);
            ST_SC1(sv, base2 + 1024);
        }
        {
            float s1 = 0.f, s2 = 0.f, t1 = 0.f, t2 = 0.f;
#pragma unroll
            for (int k = 0; k < 4; ++k) {
                s1 = dot2(xa0[k], 0x3f803f80u, s1); t1 = dot2(xa1[k], 0x3f803f80u, t1);
                s2 = dot2(xa0[k], xa0[k], s2); t2 = dot2(xa1[k], xa1[k], t2);
            }
            s1 = row_sum(s1 + t1); s2 = row_sum(s2 + t2);
            s1 = sw16_add(s1, s1); s2 = sw16_add(s2, s2);
            const float mean = s1 * (1.0f / 512.0f);
            const float var = fmaxf(fmaf(-mean, mean, s2 * (1.0f / 512.0f)), 0.f);
            const float rstd = __builtin_amdgcn_rsqf(var + 1e-5f), nb = -mean * rstd;
            v4u o0, o1;
#pragma unroll
            for (int k = 0; k < 4; ++k) {
                o0[k] = pk_bf16(fmaf(bflo(xa0[k]), rstd, nb), fmaf(bfhi(xa0[k]), rstd, nb));
                o1[k] = pk_bf16(fmaf(bflo(xa1[k]), rstd, nb), fmaf(bfhi(xa1[k]), rstd, nb));
            }
            *(v4u*)&s_xn[16 * lane] = o0;
            *(v4u*)&s_xn[16 * lane + 8] = o1;
        }
        BAR();
        const u64 tag = epoch + 1ull;
        const unsigned tg0 = (unsigned)tag, tg1 = (unsigned)(tag >> 32), thi8 = (unsigned)(tag >> 8), tlo8 = tg0 << 24;
        asm volatile("" :: "v"(thi8), "v"(tlo8));
        BAR();
        {
            const int c = lane & 31, pos = (lane >> 5) * 64 + ((c >> 1) & 1) * 32 + (c & 1) * 16 + (c >> 2);
            float q0 = 0.f, q1 = 0.f, h0 = 0.f, h1 = 0.f;
#pragma unroll
            for (int k = 0; k < 8; ++k) { q0 += s_qred[k * 128 + pos]; q1 += s_qred[k * 128 + pos + 8]; }
            ST_SC1((v4u{tlo8 | (__float_as_uint(q0) >> 8), thi8, tlo8 | (__float_as_uint(q1) >> 8), thi8}),
                   WS_QA + (kvh * 8 + qline) * 4096 + rho * 1024 + mi * 256 + r16 * 16);
            CFENCE();
#pragma unroll
            for (int k = 0; k < 8; ++k) { h0 += s_hred[k * 128 + pos]; h1 += s_hred[k * 128 + pos + 8]; }
            if (m1_on)
                ST_SC1((v4u{tlo8 | (__float_as_uint(h0) >> 8), thi8, tlo8 | (__float_as_uint(h1) >> 8), thi8}),
                       WS_HG + ((lane >> 5) * 128 + 4 * mj + (c >> 3)) * 1024 + mks * 128 + (c & 7) * 16);
        }
        __builtin_amdgcn_s_sleep(48);
        {
            v4u ga;
            for (unsigned spins = 0;; ++spins) {
                CFENCE();
                ga = LD_SC1(WS_QA + (kvh * 8 + qline) * 4096 + mi * 1024 + lane * 16);
                const unsigned bad = (ga.y ^ thi8) | (ga.w ^ thi8) | (((ga.x ^ tlo8) | (ga.z ^ tlo8)) >> 24);
                if (__all(bad == 0u) || spins > SPIN_CAP) break;
                __builtin_amdgcn_s_sleep(4);
            }
            asm volatile("s_waitcnt vmcnt(0)" ::: "memory");
            float qa = __uint_as_float(ga.x << 8), qb = __uint_as_float(ga.z << 8);
            qa = sw16_add(qa, qa); qb = sw16_add(qb, qb);
            qa = sw32_add(qa, qa); qb = sw32_add(qb, qb);
            const unsigned pk = pk_bf16(qa, qb), pk2 = __float_as_uint(dpp<XOR1>(__uint_as_float(pk)));
            if (lane < 16 && !(lane & 1)) ST_SC1((v4u{pk, tg0, pk2, tg0}), WS_QG + grp * 2048 + (2 * qline + (s >> 3)) * 128 + lane * 8);
            if (lane == 16) __hip_atomic_store((GAS u64*)(ws + WS_FLAGS) + L, tag, RLX_AGENT);
        }
        __builtin_amdgcn_s_sleep(56);
        {
            v4u g[2], gh, gf;
            unsigned bad[2] = {1u, 1u}, badh = 1u, badf = 1u;
            bool qdone = false;
            for (unsigned spins = 0;; ++spins) {
                CFENCE();
#pragma unroll
                for (int k = 0; k < 2; ++k) if (bad[k]) g[k] = LD_SC1(WS_QG + grp * 2048 + k * 1024 + lane * 16);
                if (badh) gh = LD_SC1(WS_HG + (b * 128 + hblk) * 1024 + lane * 16);
                if (badf) gf = LD_SC1(WS_FLAGS + b * 1024 + lane * 16);
#pragma unroll
                for (int k = 0; k < 2; ++k) bad[k] = (g[k].y ^ tg0) | (g[k].w ^ tg0);
                if (!qdone && (__all((bad[0] | bad[1]) == 0u) || spins > SPIN_CAP)) {
                    qdone = true;
                    *(v2u*)&s_q[2 * lane] = v2u{g[0].x, g[0].z};
                    *(v2u*)&s_q[128 + 2 * lane] = v2u{g[1].x, g[1].z};
                    LGKM0();
                    if (lane == 0) __hip_atomic_store(&s_flag, 1u, RLX_WG);
                }
                badh = (gh.y ^ thi8) | (gh.w ^ thi8) | (((gh.x ^ tlo8) | (gh.z ^ tlo8)) >> 24);
                badf = (gf.x ^ tg0) | (gf.y ^ tg1) | (gf.z ^ tg0) | (gf.w ^ tg1);
                if ((qdone && __all((badh | badf) == 0u)) || spins > SPIN_CAP) break;
                __builtin_amdgcn_s_sleep(4);
            }
            if (lane == 0) __hip_atomic_store(&s_flag, 2u, RLX_WG);
            float hv[2] = {__uint_as_float(gh.x << 8), __uint_as_float(gh.z << 8)};
#pragma unroll
            for (int k = 0; k < 2; ++k) {
                float h = hv[k];
                h += dpp<ROR8>(h); h = sw16_add(h, h); h = sw32_add(h, h);
                h += k ? bfhi(b1v) : bflo(b1v);
                const float u2 = 1.5957691216057308f * (h + 0.044715f * h * h * h);
                const float th = 1.0f - 2.0f / (__expf(u2) + 1.0f);
                hv[k] = m2_on ? 0.5f * h * (1.0f + th) : 0.f;
            }
            if (lane < 8) *(v2u*)&s_h[2 * lane] = v2u{pk_bf16(hv[0], hv[0]), pk_bf16(hv[1], hv[1])};
        }
        LDS_WAIT(s_cnt, 8u, 2);
        CFENCE();
        {
            const int h = (lane >> 3) & 3;
            const f32x2 ml = *(const f32x2*)&s_ml[(j2 * 4 + h) * 2];
            const float M = oct_max(ml.x);
            const float f = __builtin_amdgcn_exp2f(ml.x - M);
            const float Ls = oct_sum(f * ml.y);
            if (lane < 32) s_fw[lane] = f;
            if (lane < 32 && j2 < 4) {
                const v2u mlv = {min(__float_as_uint(M), SENT - 1u), min(__float_as_uint(Ls), SENT - 1u)};
                __builtin_amdgcn_raw_buffer_store_b64(mlv, rs, WS_BLK + (grp * 16 + 4 * h + j2) * BLK_SZ + BLK_ML + s * 8, 0, AUX_SC1);
            }
        }
        BAR();
        v4u k0, k1, k2, k3;
        {
            const int base = WS_BLK + L * BLK_SZ;
            const int oa = base + BLK_IN1 + (2 * j2) * 128 + (lane >> 3) * 16;
            v4u ML;
            for (unsigned spins = 0;; ++spins) {
                CFENCE();
                ML = LD_SC1(base + BLK_ML + j2 * 16);
                if (__all(max4(ML) != SENT) || spins > SPIN_CAP) break;
                __builtin_amdgcn_s_sleep(2);
            }
            const float M0 = __uint_as_float(ML.x), L0 = __uint_as_float(ML.y), M1 = __uint_as_float(ML.z), L1 = __uint_as_float(ML.w);
            const float Mx = oct_max(fmaxf(M0, M1));
            float e0 = __builtin_amdgcn_exp2f(M0 - Mx), e1 = __builtin_amdgcn_exp2f(M1 - Mx);
            const float inv = __builtin_amdgcn_rcpf(oct_sum(fmaf(e0, L0, e1 * L1)));
            e0 *= inv; e1 *= inv;
            float o4[4];
            POLL_PAIR(128, o4, k0, k1, k2, k3, oa, srd, e0, e1);
            v4u ob;
#pragma unroll
            for (int t = 0; t < 4; ++t) { const float o = oct_sum(o4[t]); ob[t] = pk_bf16(o, o); }
            if (j2 == 0) *(v4u*)&s_o[(lane >> 3) * 4] = ob;
        }
        BAR();
        POLL_DRAIN(k0, k1, k2, k3);
        if (fin_on) {
            BAR();
            float r = bflo(xo) + bflo(b2o);
#pragma unroll
            for (int k = 0; k < 8; ++k) r += s_fin[k * 32 + (lane & 31)];
            if (lane < 32) out[b * 512 + ocol] = (u16)(pk_bf16(r, r) & 0xffffu);
            if (L == 0 && lane == 0) __hip_atomic_store((GAS u64*)(ws + WS_EPOCH), tag, RLX_AGENT);
        }
        return;
    }

    v4u wqr[2], w1r;
    {
        const u16* p = wq + (size_t)(128 * mi + 16 * w) * 4096 + kvh * 512 + qline * 64;
        const unsigned lo = (unsigned)(lane >> 3) * 4096u + 8u * j2;
        wqr[0] = ld_nt(p + lo);
        wqr[1] = ld_nt(p + lo + 8 * 4096);
        CFENCE();
        w1r = ld_nt(w1 + (size_t)(64 * mks + 8 * w) * 1792 + mj * 64 + ((unsigned)(lane >> 3) * 1792u + 8u * j2));
    }
    CFENCE(); SB();
    BAR();
    const unsigned char* kb = (const unsigned char*)Kc + (size_t)grp * 8192 * 256;
    const unsigned char* vb = (const unsigned char*)Vc + (size_t)grp * 8192 * 256;
    const unsigned klo = (4u * (r16 & 3) + ((r16 >> 2) & 1)) * 256u + 64u * (r16 >> 3) + 16u * rho;
    const unsigned vlo = 16u * lane;
#define KIN(i) ((((i) >> 1) & 1) * 512 + ((i) & 1) * 128)
#define TOFF(u) ((unsigned)(((u) * 128 + s * 8 + w + 32 * grp) & 511) * 4096u)
    v4u kr[16], vr[16];
#pragma unroll
    for (int i = 0; i < K0; ++i) kr[i] = ld_nt(kb + TOFF(i >> 2) + klo + KIN(i));
    CFENCE(); SB();
    const f32x4 z4 = {0.f, 0.f, 0.f, 0.f};
    BAR();
    {
        const int xi = ((lane >> 2) & 1) * 512 + 2 * rho + ((lane >> 3) & 1);
        const unsigned sh = (lane & 1) * 16;
        const unsigned e0 = (unsigned)s_xn[xi + 128 * mi + 16 * w] << sh, e1 = (unsigned)s_xn[xi + 128 * mi + 16 * w + 8] << sh;
        const unsigned e2 = (unsigned)s_xn[xi + 64 * mks + 8 * w] << sh;
        const bool lo2 = (lane & 2) == 0;
        const unsigned a0 = lo2 ? e0 : 0u, c0 = lo2 ? 0u : e0, a1 = lo2 ? e1 : 0u, c1 = lo2 ? 0u : e1, a2 = lo2 ? e2 : 0u, c2_ = lo2 ? 0u : e2;
        const f32x4 d0 = mfma16(wqr[1].x, wqr[1].y, a1, c1, mfma16(wqr[0].x, wqr[0].y, a0, c0, z4));
        const f32x4 d1 = mfma16(wqr[1].z, wqr[1].w, a1, c1, mfma16(wqr[0].z, wqr[0].w, a0, c0, z4));
        f32x4 u;
#pragma unroll
        for (int i = 0; i < 4; ++i) {
            auto r = __builtin_amdgcn_permlane32_swap(__float_as_uint(d0[i]), __float_as_uint(d1[i]), false, false);
            u[i] = __uint_as_float(r[0]) + dpp<ROR8>(__uint_as_float(r[1]));
        }
        const int pos = w * 128 + ((lane >> 2) & 1) * 64 + (rho >> 1) * 32 + (lane & 3) * 8 + (rho & 1) * 4;
        if ((lane & 8) == 0) *(f32x4*)&s_qred[pos] = u;
        const f32x4 g0 = mfma16(w1r.x, w1r.y, a2, c2_, z4), g1 = mfma16(w1r.z, w1r.w, a2, c2_, z4);
#pragma unroll
        for (int i = 0; i < 4; ++i) {
            auto r = __builtin_amdgcn_permlane32_swap(__float_as_uint(g0[i]), __float_as_uint(g1[i]), false, false);
            u[i] = __uint_as_float(r[0]) + dpp<ROR8>(__uint_as_float(r[1]));
        }
        if ((lane & 8) == 0) *(f32x4*)&s_hred[pos] = u;
    }
    BAR();
    unsigned qrdy = 0;
#define WAITW() do { if (!qrdy) { qrdy = __builtin_amdgcn_readfirstlane(__hip_atomic_load(&s_flag, RLX_WG)) >> 1; \
        if (!qrdy) asm volatile("s_waitcnt vmcnt(2)" ::: "memory"); } } while (0)
#pragma unroll
    for (int i = K0; i < 16; ++i) { WAITW(); kr[i] = ld_nt(kb + TOFF(i >> 2) + klo + KIN(i)); CFENCE(); }
#pragma unroll
    for (int i = 0; i < 16; ++i) { WAITW(); vr[i] = ld_nt(vb + TOFF(i >> 2) + vlo + (i & 3) * 1024); CFENCE(); }
    if (!qrdy) LDS_WAIT(s_flag, 1u, 1);
    CFENCE(); SB();
    constexpr float C2 = 0.08838834764831845f * 1.4426950408889634f;
    unsigned pp[4][4];
    {
        f32x4 S[4];
        v4u qf[2];
#pragma unroll
        for (int hf = 0; hf < 2; ++hf) qf[hf] = *(const v4u*)&s_q[(r16 >> 2) * 64 + 32 * hf + 16 * ((r16 >> 1) & 1) + 4 * rho];
#pragma unroll
        for (int u = 0; u < 4; ++u) {
            const f32x4 da = mfma32(kr[u * 4 + 1], qf[1], mfma32(kr[u * 4 + 0], qf[0], z4));
            const f32x4 db = mfma32(kr[u * 4 + 3], qf[1], mfma32(kr[u * 4 + 2], qf[0], z4));
#pragma unroll
            for (int i = 0; i < 4; ++i) {
                auto r = __builtin_amdgcn_permlane32_swap(__float_as_uint(da[i]), __float_as_uint(db[i]), false, false);
                S[u][i] = dpp<BC0>(__uint_as_float(r[0])) + dpp<BC2>(__uint_as_float(r[1]));
            }
        }
        float m = S[0][0];
#pragma unroll
        for (int u = 0; u < 4; ++u)
#pragma unroll
            for (int i = 0; i < 4; ++i) m = fmaxf(m, S[u][i]);
        m = sw16_max(m); m = sw32_max(m);
        m *= C2;
        float l4[4] = {0.f, 0.f, 0.f, 0.f};
#pragma unroll
        for (int u = 0; u < 4; ++u)
#pragma unroll
            for (int i = 0; i < 4; ++i) { const float p = __builtin_amdgcn_exp2f(fmaf(S[u][i], C2, -m)); l4[i] += p; pp[u][i] = pk_bf16(p, p); }
        float l = (l4[0] + l4[1]) + (l4[2] + l4[3]);
        l = sw16_add(l, l); l = sw32_add(l, l);
        if (lane < 16 && (lane & 3) == 0) *(f32x2*)&s_ml[(w * 4 + (lane >> 2)) * 2] = f32x2{m, l};
    }
    LGKM0();
    if (lane == 0) __hip_atomic_fetch_add(&s_cnt, 1u, RLX_WG);
    {
        const unsigned hm = (lane & 1) ? 0xffff0000u : 0x0000ffffu;
        const unsigned ma = (lane & 2) ? 0u : hm, mb = (lane & 2) ? hm : 0u;
        f32x4 O0 = z4, O1 = z4;
#pragma unroll
        for (int u = 0; u < 4; ++u)
#pragma unroll
            for (int i = 0; i < 4; ++i) {
                const unsigned pa = pp[u][i] & ma, pb = pp[u][i] & mb;
                const v4u v = vr[u * 4 + i];
                O0 = mfma16(v.x, v.y, pa, pb, O0);
                O1 = mfma16(v.z, v.w, pa, pb, O1);
            }
        float* po = &s_ored[(w * 128 + (r16 >> 2) * 32 + rho * 8 + (lane & 3)) * 4];
        *(f32x4*)po = O0;
        *(f32x4*)(po + 16) = O1;
    }
    int o_pub1 = WS_BLK + (grp * 16 + (tid >> 3)) * BLK_SZ + BLK_IN1 + s * 128 + j2 * 16;
    int o_pub2 = WS_IN2 + (b * 16 + 2 * w + rho) * 16384 + c2 * 128 + j2 * 16;
    int o_fin = WS_IN2 + (b * 16 + fj) * 16384 + w * 2048 + lane * 16;
    asm volatile("" : "+v"(o_pub1), "+v"(o_pub2), "+v"(o_fin));
    CFENCE(); SB();
    BAR();
    if (w < 2) {
        const f32x4 fa = *(const f32x4*)&s_fw[(tid >> 5) * 8], fb = *(const f32x4*)&s_fw[(tid >> 5) * 8 + 4];
        f32x4 acc = fa[0] * *(const f32x4*)&s_ored[tid * 4];
#pragma unroll
        for (int k = 1; k < 8; ++k) acc += (k < 4 ? fa[k & 3] : fb[k & 3]) * *(const f32x4*)&s_ored[(k * 128 + tid) * 4];
        ST_SC1(nosent(acc), o_pub1);
    }
    CFENCE();
    v4u wor[4], w2r[2];
    {
        const u16* p = wo + (size_t)(kvh * 512 + s * 32) * 512 + 64 * w;
        const u16* p2 = w2 + (size_t)hblk * 16 * 512 + 64 * w;
        const unsigned lo = (unsigned)(lane >> 3) * 512u + 8u * j2;
        w2r[0] = *(const v4u*)(p2 + lo); w2r[1] = *(const v4u*)(p2 + lo + 8 * 512);
#pragma unroll
        for (int t = 0; t < 4; ++t) wor[t] = *(const v4u*)(p + lo + t * 8 * 512);
    }
    CFENCE(); SB();
    unsigned msk[4];
#pragma unroll
    for (int k = 0; k < 4; ++k) msk[k] = (k == (j2 >> 1)) ? ((j2 & 1) ? 0xffff0000u : 0x0000ffffu) : 0u;
    const unsigned ph0 = s_h[lane >> 3], ph1 = s_h[(lane >> 3) + 8];
    v4u bh0 = {ph0 & msk[0], ph0 & msk[1], ph0 & msk[2], ph0 & msk[3]}, bh1 = {ph1 & msk[0], ph1 & msk[1], ph1 & msk[2], ph1 & msk[3]};
    asm volatile("" : "+v"(bh0), "+v"(bh1));
    BAR();
    {
        const v4u ob = *(const v4u*)&s_o[(lane >> 3) * 4];
        v4u bo[4];
#pragma unroll
        for (int t = 0; t < 4; ++t) bo[t] = v4u{ob[t] & msk[0], ob[t] & msk[1], ob[t] & msk[2], ob[t] & msk[3]};
        asm volatile("" : "+v"(bo[0]), "+v"(bo[1]), "+v"(bo[2]), "+v"(bo[3]));
        f32x4 Y = mfma32(w2r[1], bh1, mfma32(w2r[0], bh0, z4));
#pragma unroll
        for (int t = 0; t < 4; ++t) Y = mfma32(wor[t], bo[t], Y);
        f32x4 Z;
#pragma unroll
        for (int i = 0; i < 4; ++i) {
            const float y = dpp<ROR8>(Y[i]);
            auto r = __builtin_amdgcn_permlane32_swap(__float_as_uint(y), __float_as_uint(y), false, false);
            Z[i] = Y[i] + __uint_as_float(r[1]);
        }
        if (lane < 32 && (lane & 8) == 0) ST_SC1(nosent(Z), o_pub2);
    }
    if (fin_on) {
        v4u k0, k1, k2, k3;
        float v[4];
        const float one = 1.0f;
        __builtin_amdgcn_s_sleep(10);
        POLL_PAIR(1024, v, k0, k1, k2, k3, o_fin, srd, one, one);
        const float t0 = sw32_add(v[0], v[1]), t1 = sw32_add(v[2], v[3]);
        float r = sw16_add(t0, t1);
        r += dpp<ROR8>(r);
        if ((lane & 8) == 0) s_fin[w * 32 + 8 * (2 * (rho & 1) + (rho >> 1)) + j2] = r;
        BAR();
        POLL_DRAIN(k0, k1, k2, k3);
    }
}

extern "C" void kernel_launch(void* const* d_in, const int* in_sizes, int n_in, void* d_out, int out_size, void* d_ws, size_t ws_size,
                              hipStream_t stream) {
    fused_decode<<<256, 576, 0, stream>>>((const u16*)d_in[0], (const u16*)d_in[1], (const u16*)d_in[2], (const u16*)d_in[3], (const u16*)d_in[4],
                                          (const u16*)d_in[6], (const u16*)d_in[7], (const u16*)d_in[8], (const u16*)d_in[9], (u16*)d_out,
                                          (unsigned char*)d_ws);
}
```

```cpp
#define PROBE 1
#include <hip/hip_runtime.h>
#include <stdint.h>

typedef unsigned short u16;
typedef unsigned long long u64;
typedef unsigned v4u __attribute__((ext_vector_type(4)));
typedef unsigned v2u __attribute__((ext_vector_type(2)));
typedef float f32x4 __attribute__((ext_vector_type(4)));
typedef float f32x2 __attribute__((ext_vector_type(2)));
typedef short s16x4 __attribute__((ext_vector_type(4)));
typedef __bf16 bf16x8 __attribute__((ext_vector_type(8)));
typedef __bf16 bf16x2 __attribute__((ext_vector_type(2)));
#define GAS __attribute__((address_space(1)))
#define RLX_AGENT __ATOMIC_RELAXED, __HIP_MEMORY_SCOPE_AGENT
#define RLX_WG __ATOMIC_RELAXED, __HIP_MEMORY_SCOPE_WORKGROUP

constexpr int K0 = 5;
constexpr int WS_EPOCH = 0;
constexpr int WS_FLAGS = 4096;
constexpr int WS_QG = 8192;
constexpr int WS_BLK = 131072;
constexpr int BLK_SZ = 3072, BLK_IN1 = 0, BLK_ML = 2048;
constexpr int WS_IN2 = WS_BLK + 256 * BLK_SZ;
constexpr int WS_QA = WS_IN2 + 32 * 16384;
constexpr int WS_HG = WS_QA + 64 * 4096;
constexpr int WS_BYTES = WS_HG + 2 * 128 * 1024;
constexpr unsigned SENT = 0xFFFFFFFFu;
constexpr int AUX_SC1 = 16;
constexpr unsigned SPIN_CAP = 1u << 18;

template <int CTRL> __device__ __forceinline__ float dpp(float x) {
    return __builtin_bit_cast(float, __builtin_amdgcn_mov_dpp(__builtin_bit_cast(int, x), CTRL, 0xf, 0xf, true));
}
constexpr int XOR1 = 0xB1, XOR2 = 0x4E, HMIR = 0x141, ROR8 = 0x128, BC0 = 0x00, BC2 = 0xAA;
__device__ __forceinline__ float sw32_add(float a, float b) {
    auto r = __builtin_amdgcn_permlane32_swap(__float_as_uint(a), __float_as_uint(b), false, false);
    return __uint_as_float(r[0]) + __uint_as_float(r[1]);
}
__device__ __forceinline__ float sw16_add(float a, float b) {
    auto r = __builtin_amdgcn_permlane16_swap(__float_as_uint(a), __float_as_uint(b), false, false);
    return __uint_as_float(r[0]) + __uint_as_float(r[1]);
}
__device__ __forceinline__ float sw32_max(float a) {
    auto r = __builtin_amdgcn_permlane32_swap(__float_as_uint(a), __float_as_uint(a), false, false);
    return fmaxf(__uint_as_float(r[0]), __uint_as_float(r[1]));
}
__device__ __forceinline__ float sw16_max(float a) {
    auto r = __builtin_amdgcn_permlane16_swap(__float_as_uint(a), __float_as_uint(a), false, false);
    return fmaxf(__uint_as_float(r[0]), __uint_as_float(r[1]));
}
__device__ __forceinline__ float oct_sum(float x) { x += dpp<XOR1>(x); x += dpp<XOR2>(x); x += dpp<HMIR>(x); return x; }
__device__ __forceinline__ float oct_max(float x) { x = fmaxf(x, dpp<XOR1>(x)); x = fmaxf(x, dpp<XOR2>(x)); x = fmaxf(x, dpp<HMIR>(x)); return x; }
__device__ __forceinline__ float row_sum(float x) { x = oct_sum(x); x += dpp<ROR8>(x); return x; }
__device__ __forceinline__ float bflo(unsigned u) { return __uint_as_float(u << 16); }
__device__ __forceinline__ float bfhi(unsigned u) { return __uint_as_float(u & 0xffff0000u); }
__device__ __forceinline__ unsigned pk_bf16(float lo, float hi) {
    f32x2 v = {lo, hi};
    return __builtin_bit_cast(unsigned, __builtin_convertvector(v, bf16x2));
}
__device__ __forceinline__ float dot2(unsigned a, unsigned b, float c) {
    return __builtin_amdgcn_fdot2_f32_bf16(__builtin_bit_cast(bf16x2, a), __builtin_bit_cast(bf16x2, b), c, false);
}
__device__ __forceinline__ f32x4 mfma32(v4u a, v4u b, f32x4 c) {
    return __builtin_amdgcn_mfma_f32_16x16x32_bf16(__builtin_bit_cast(bf16x8, a), __builtin_bit_cast(bf16x8, b), c, 0, 0, 0);
}
__device__ __forceinline__ f32x4 mfma16(unsigned a0, unsigned a1, unsigned b0, unsigned b1, f32x4 c) {
    return __builtin_amdgcn_mfma_f32_16x16x16bf16_1k(__builtin_bit_cast(s16x4, v2u{a0, a1}), __builtin_bit_cast(s16x4, v2u{b0, b1}), c, 0, 0, 0);
}
__device__ __forceinline__ v4u ld_nt(const void* p) { return __builtin_nontemporal_load((const v4u*)p); }
__device__ __forceinline__ v4u nosent(f32x4 v) {
    v4u u = __builtin_bit_cast(v4u, v);
    u.x = min(u.x, SENT - 1u); u.y = min(u.y, SENT - 1u); u.z = min(u.z, SENT - 1u); u.w = min(u.w, SENT - 1u);
    return u;
}
__device__ __forceinline__ unsigned max4(v4u a) { return max(max(a.x, a.y), max(a.z, a.w)); }

#define POLL_CHECK(b0, b1, b2, b3, b4, b5, b6, b7) \
    "v_max3_u32 v144, v" #b0 ", v" #b1 ", v" #b2 "\n\tv_max3_u32 v145, v" #b3 ", v" #b4 ", v" #b5 "\n\t" \
    "v_max3_u32 v144, v144, v" #b6 ", v" #b7 "\n\tv_max_u32 v144, v144, v145\n\tv_cmp_eq_u32 vcc, -1, v144\n\t" \
    "v_mul_f32 %0, %11, v" #b0 "\n\tv_mul_f32 %1, %11, v" #b1 "\n\tv_mul_f32 %2, %11, v" #b2 "\n\tv_mul_f32 %3, %11, v" #b3 "\n\t" \
    "v_fmac_f32 %0, %12, v" #b4 "\n\tv_fmac_f32 %1, %12, v" #b5 "\n\tv_fmac_f32 %2, %12, v" #b6 "\n\tv_fmac_f32 %3, %12, v" #b7 "\n\t" \
    "s_mov_b64 exec, vcc\n\t" \
    "s_cbranch_vccz 2f\n\t"
#define POLL_PAIR(D, r, k0, k1, k2, k3, off, srd, e0, e1) do { unsigned cnt_; u64 ex_; \
    asm volatile( \
        "s_mov_b64 %9, exec\n\t" \
        "buffer_load_dwordx4 v[128:131], %10, %13, 0 offen sc1\n\t" \
        "buffer_load_dwordx4 v[132:135], %10, %13, 0 offen offset:" #D " sc1\n\t" \
        "s_mov_b32 %4, 0x40000\n\t" \
        "s_sleep 8\n" \
        "1:\n\t" \
        "buffer_load_dwordx4 v[136:139], %10, %13, 0 offen sc1\n\t" \
        "buffer_load_dwordx4 v[140:143], %10, %13, 0 offen offset:" #D " sc1\n\t" \
        "s_waitcnt vmcnt(2)\n\t" \
        POLL_CHECK(128, 129, 130, 131, 132, 133, 134, 135) \
        "buffer_load_dwordx4 v[128:131], %10, %13, 0 offen sc1\n\t" \
        "buffer_load_dwordx4 v[132:135], %10, %13, 0 offen offset:" #D " sc1\n\t" \
        "s_waitcnt vmcnt(2)\n\t" \
        POLL_CHECK(136, 137, 138, 139, 140, 141, 142, 143) \
        "s_sub_u32 %4, %4, 1\n\t" \
        "s_cbranch_scc0 1b\n" \
        "2:\n\t" \
        "s_mov_b64 exec, %9\n\t" \
        "s_nop 4" \
        : "=&v"(r[0]), "=&v"(r[1]), "=&v"(r[2]), "=&v"(r[3]), "=&s"(cnt_), \
          "=&{v[128:131]}"(k0), "=&{v[132:135]}"(k1), "=&{v[136:139]}"(k2), "=&{v[140:143]}"(k3), "=&s"(ex_) \
        : "v"(off), "v"(e0), "v"(e1), "s"(srd) \
        : "v144", "v145", "vcc", "scc", "memory"); } while (0)
#define POLL_DRAIN(k0, k1, k2, k3) \
    asm volatile("s_waitcnt vmcnt(0)" :: "{v[128:131]}"(k0), "{v[132:135]}"(k1), "{v[136:139]}"(k2), "{v[140:143]}"(k3) : "memory")
#define SB() __builtin_amdgcn_sched_barrier(0)
#define CFENCE() asm volatile("" ::: "memory")
#define LGKM0() asm volatile("s_waitcnt lgkmcnt(0)" ::: "memory")
#define BAR() do { asm volatile("s_waitcnt lgkmcnt(0)" ::: "memory"); __builtin_amdgcn_s_barrier(); asm volatile("" ::: "memory"); } while (0)
#define LDS_WAIT(word, target, slp) \
    for (unsigned spins_ = 0; __hip_atomic_load(&(word), RLX_WG) < (target) && spins_ < (SPIN_CAP << 4); ++spins_) __builtin_amdgcn_s_sleep(slp)
#define LD_SC1(off) __builtin_amdgcn_raw_buffer_load_b128(rs, (off), 0, AUX_SC1)
#define ST_SC1(v, off) __builtin_amdgcn_raw_buffer_store_b128((v), rs, (off), 0, AUX_SC1)

__global__ void __launch_bounds__(576, 3)
fused_decode(const u16* __restrict__ x, const u16* __restrict__ Kc, const u16* __restrict__ Vc, const u16* __restrict__ wq,
             const u16* __restrict__ wo, const u16* __restrict__ w1, const u16* __restrict__ b1, const u16* __restrict__ w2,
             const u16* __restrict__ b2, u16* __restrict__ out, unsigned char* __restrict__ ws) {
    __shared__ __attribute__((aligned(16))) float s_ored[8 * 128 * 4];
    __shared__ __attribute__((aligned(16))) float s_qred[8 * 128];
    __shared__ __attribute__((aligned(16))) float s_hred[8 * 128];
    __shared__ __attribute__((aligned(16))) unsigned s_q[256];
    __shared__ __attribute__((aligned(16))) u16 s_xn[2 * 512];
    __shared__ __attribute__((aligned(16))) unsigned s_h[16];
    __shared__ __attribute__((aligned(16))) unsigned s_o[32];
    __shared__ __attribute__((aligned(16))) float s_ml[8 * 4 * 2];
    __shared__ __attribute__((aligned(16))) float s_fw[4 * 8];
    __shared__ __attribute__((aligned(16))) float s_fin[8 * 32];
    __shared__ unsigned s_flag, s_cnt;

    asm volatile("" :: "s"(x), "s"(Kc), "s"(Vc), "s"(wq), "s"(wo), "s"(w1), "s"(b1), "s"(w2), "s"(b2), "s"(out), "s"(ws));
    const int L = blockIdx.x;
    const int b = L >> 7, kvh = (L >> 4) & 7, s = L & 15, grp = L >> 4, c2 = L & 127;
    const int qline = (s + kvh) & 7;
    const int mi = 2 * b + (s >> 3);
    const int lane = threadIdx.x & 63, w0 = __builtin_amdgcn_readfirstlane(threadIdx.x >> 6);
    const int w = w0 - 1, tid = w * 64 + lane;
    const int rho = lane >> 4, j2 = lane & 7;
    const unsigned r16 = lane & 15;
    const __amdgpu_buffer_rsrc_t rs = __builtin_amdgcn_make_buffer_rsrc(ws, 0, WS_BYTES, 0x00020000);
    const v4u srd = {(unsigned)(uintptr_t)ws, (unsigned)((uintptr_t)ws >> 32) & 0xffffu, (unsigned)WS_BYTES, 0x00020000u};
    const bool m1_on = L < 224, m2_on = c2 < 112;
    const int mj = m1_on ? L % 28 : L - 224, mks = m1_on ? L / 28 : 0;
    const int hblk = m2_on ? c2 : c2 - 16;
    const bool fin_on = (s & 7) == kvh;
    const int fj = 2 * kvh + (s >> 3);

    if (w0 == 0) {
        __builtin_amdgcn_s_setprio(3);
        const u64 pt0 = __builtin_amdgcn_s_memrealtime(); u64 pt1 = pt0;
        const v4u xa0 = *(const v4u*)(x + 16 * lane), xa1 = *(const v4u*)(x + 16 * lane + 8);
        CFENCE();
        const u64 epoch = __hip_atomic_load((GAS u64*)(ws + WS_EPOCH), RLX_AGENT);
        if (lane == 0) { s_flag = 0; s_cnt = 0; }
        BAR();
        const unsigned b1v = *(const unsigned*)(b1 + hblk * 16 + 2 * j2);
        const int ocol = 32 * fj + (lane & 31);
        const unsigned xo = x[b * 512 + ocol], b2o = b2[ocol];
        CFENCE();
        {
            const v4u sv = {SENT, SENT, SENT, SENT};
            const int base = WS_BLK + L * BLK_SZ + lane * 16, base2 = WS_IN2 + L * 2048 + lane * 16;
            ST_SC1(sv, base);
            ST_SC1(sv, base + 1024);
            ST_SC1(sv, lane < 8 ? base + 2048 : 0x7ffffff0);
            ST_SC1(sv, base2);
            ST_SC1(sv, base2 + 1024);
        }
        {
            float s1 = 0.f, s2 = 0.f, t1 = 0.f, t2 = 0.f;
#pragma unroll
            for (int k = 0; k < 4; ++k) {
                s1 = dot2(xa0[k], 0x3f803f80u, s1); t1 = dot2(xa1[k], 0x3f803f80u, t1);
                s2 = dot2(xa0[k], xa0[k], s2); t2 = dot2(xa1[k], xa1[k], t2);
            }
            s1 = row_sum(s1 + t1); s2 = row_sum(s2 + t2);
            s1 = sw16_add(s1, s1); s2 = sw16_add(s2, s2);
            const float mean = s1 * (1.0f / 512.0f);
            const float var = fmaxf(fmaf(-mean, mean, s2 * (1.0f / 512.0f)), 0.f);
            const float rstd = __builtin_amdgcn_rsqf(var + 1e-5f), nb = -mean * rstd;
            v4u o0, o1;
#pragma unroll
            for (int k = 0; k < 4; ++k) {
                o0[k] = pk_bf16(fmaf(bflo(xa0[k]), rstd, nb), fmaf(bfhi(xa0[k]), rstd, nb));
                o1[k] = pk_bf16(fmaf(bflo(xa1[k]), rstd, nb), fmaf(bfhi(xa1[k]), rstd, nb));
            }
            *(v4u*)&s_xn[16 * lane] = o0;
            *(v4u*)&s_xn[16 * lane + 8] = o1;
        }
        BAR();
        const u64 tag = epoch + 1ull;
        const unsigned tg0 = (unsigned)tag, tg1 = (unsigned)(tag >> 32), thi8 = (unsigned)(tag >> 8), tlo8 = tg0 << 24;
        asm volatile("" :: "v"(thi8), "v"(tlo8));
        BAR();
        if (PROBE == 1) pt1 = __builtin_amdgcn_s_memrealtime();
        {
            const int c = lane & 31, pos = (lane >> 5) * 64 + ((c >> 1) & 1) * 32 + (c & 1) * 16 + (c >> 2);
            float q0 = 0.f, q1 = 0.f, h0 = 0.f, h1 = 0.f;
#pragma unroll
            for (int k = 0; k < 8; ++k) { q0 += s_qred[k * 128 + pos]; q1 += s_qred[k * 128 + pos + 8]; }
            ST_SC1((v4u{tlo8 | (__float_as_uint(q0) >> 8), thi8, tlo8 | (__float_as_uint(q1) >> 8), thi8}),
                   WS_QA + (kvh * 8 + qline) * 4096 + rho * 1024 + mi * 256 + r16 * 16);
            CFENCE();
#pragma unroll
            for (int k = 0; k < 8; ++k) { h0 += s_hred[k * 128 + pos]; h1 += s_hred[k * 128 + pos + 8]; }
            if (m1_on)
                ST_SC1((v4u{tlo8 | (__float_as_uint(h0) >> 8), thi8, tlo8 | (__float_as_uint(h1) >> 8), thi8}),
                       WS_HG + ((lane >> 5) * 128 + 4 * mj + (c >> 3)) * 1024 + mks * 128 + (c & 7) * 16);
        }
        __builtin_amdgcn_s_sleep(48);
        {
            v4u ga;
            for (unsigned spins = 0;; ++spins) {
                CFENCE();
                ga = LD_SC1(WS_QA + (kvh * 8 + qline) * 4096 + mi * 1024 + lane * 16);
                const unsigned bad = (ga.y ^ thi8) | (ga.w ^ thi8) | (((ga.x ^ tlo8) | (ga.z ^ tlo8)) >> 24);
                if (__all(bad == 0u) || spins > SPIN_CAP) break;
                __builtin_amdgcn_s_sleep(4);
            }
            asm volatile("s_waitcnt vmcnt(0)" ::: "memory");
            float qa = __uint_as_float(ga.x << 8), qb = __uint_as_float(ga.z << 8);
            qa = sw16_add(qa, qa); qb = sw16_add(qb, qb);
            qa = sw32_add(qa, qa); qb = sw32_add(qb, qb);
            const unsigned pk = pk_bf16(qa, qb), pk2 = __float_as_uint(dpp<XOR1>(__uint_as_float(pk)));
            if (lane < 16 && !(lane & 1)) ST_SC1((v4u{pk, tg0, pk2, tg0}), WS_QG + grp * 2048 + (2 * qline + (s >> 3)) * 128 + lane * 8);
            if (lane == 16) __hip_atomic_store((GAS u64*)(ws + WS_FLAGS) + L, tag, RLX_AGENT);
        }
        __builtin_amdgcn_s_sleep(56);
        {
            v4u g[2], gh, gf;
            unsigned bad[2] = {1u, 1u}, badh = 1u, badf = 1u;
            bool qdone = false;
            for (unsigned spins = 0;; ++spins) {
                CFENCE();
#pragma unroll
                for (int k = 0; k < 2; ++k) if (bad[k]) g[k] = LD_SC1(WS_QG + grp * 2048 + k * 1024 + lane * 16);
                if (badh) gh = LD_SC1(WS_HG + (b * 128 + hblk) * 1024 + lane * 16);
                if (badf) gf = LD_SC1(WS_FLAGS + b * 1024 + lane * 16);
#pragma unroll
                for (int k = 0; k < 2; ++k) bad[k] = (g[k].y ^ tg0) | (g[k].w ^ tg0);
                if (!qdone && (__all((bad[0] | bad[1]) == 0u) || spins > SPIN_CAP)) {
                    qdone = true;
                    *(v2u*)&s_q[2 * lane] = v2u{g[0].x, g[0].z};
                    *(v2u*)&s_q[128 + 2 * lane] = v2u{g[1].x, g[1].z};
                    LGKM0();
                    if (lane == 0) __hip_atomic_store(&s_flag, 1u, RLX_WG);
                }
                badh = (gh.y ^ thi8) | (gh.w ^ thi8) | (((gh.x ^ tlo8) | (gh.z ^ tlo8)) >> 24);
                badf = (gf.x ^ tg0) | (gf.y ^ tg1) | (gf.z ^ tg0) | (gf.w ^ tg1);
                if ((qdone && __all((badh | badf) == 0u)) || spins > SPIN_CAP) break;
                __builtin_amdgcn_s_sleep(4);
            }
            if (PROBE == 3) pt1 = __builtin_amdgcn_s_memrealtime();
            if (lane == 0) __hip_atomic_store(&s_flag, 2u, RLX_WG);
            float hv[2] = {__uint_as_float(gh.x << 8), __uint_as_float(gh.z << 8)};
#pragma unroll
            for (int k = 0; k < 2; ++k) {
                float h = hv[k];
                h += dpp<ROR8>(h); h = sw16_add(h, h); h = sw32_add(h, h);
                h += k ? bfhi(b1v) : bflo(b1v);
                const float u2 = 1.5957691216057308f * (h + 0.044715f * h * h * h);
                const float th = 1.0f - 2.0f / (__expf(u2) + 1.0f);
                hv[k] = m2_on ? 0.5f * h * (1.0f + th) : 0.f;
            }
            if (lane < 8) *(v2u*)&s_h[2 * lane] = v2u{pk_bf16(hv[0], hv[0]), pk_bf16(hv[1], hv[1])};
        }
        LDS_WAIT(s_cnt, 8u, 2);
        CFENCE();
        {
            const int h = (lane >> 3) & 3;
            const f32x2 ml = *(const f32x2*)&s_ml[(j2 * 4 + h) * 2];
            const float M = oct_max(ml.x);
            const float f = __builtin_amdgcn_exp2f(ml.x - M);
            const float Ls = oct_sum(f * ml.y);
            if (lane < 32) s_fw[lane] = f;
            if (lane < 32 && j2 < 4) {
                const v2u mlv = {min(__float_as_uint(M), SENT - 1u), min(__float_as_uint(Ls), SENT - 1u)};
                __builtin_amdgcn_raw_buffer_store_b64(mlv, rs, WS_BLK + (grp * 16 + 4 * h + j2) * BLK_SZ + BLK_ML + s * 8, 0, AUX_SC1);
            }
        }
        BAR();
        if (PROBE == 4) pt1 = __builtin_amdgcn_s_memrealtime();
        v4u k0, k1, k2, k3;
        {
            const int base = WS_BLK + L * BLK_SZ;
            const int oa = base + BLK_IN1 + (2 * j2) * 128 + (lane >> 3) * 16;
            v4u ML;
            for (unsigned spins = 0;; ++spins) {
                CFENCE();
                ML = LD_SC1(base + BLK_ML + j2 * 16);
                if (__all(max4(ML) != SENT) || spins > SPIN_CAP) break;
                __builtin_amdgcn_s_sleep(2);
            }
            const float M0 = __uint_as_float(ML.x), L0 = __uint_as_float(ML.y), M1 = __uint_as_float(ML.z), L1 = __uint_as_float(ML.w);
            const float Mx = oct_max(fmaxf(M0, M1));
            float e0 = __builtin_amdgcn_exp2f(M0 - Mx), e1 = __builtin_amdgcn_exp2f(M1 - Mx);
            const float inv = __builtin_amdgcn_rcpf(oct_sum(fmaf(e0, L0, e1 * L1)));
            e0 *= inv; e1 *= inv;
            float o4[4];
            POLL_PAIR(128, o4, k0, k1, k2, k3, oa, srd, e0, e1);
            v4u ob;
#pragma unroll
            for (int t = 0; t < 4; ++t) { const float o = oct_sum(o4[t]); ob[t] = pk_bf16(o, o); }
            if (j2 == 0) *(v4u*)&s_o[(lane >> 3) * 4] = ob;
        }
        BAR();
        POLL_DRAIN(k0, k1, k2, k3);
        if (fin_on) {
            BAR();
            float r = bflo(xo) + bflo(b2o);
#pragma unroll
            for (int k = 0; k < 8; ++k) r += s_fin[k * 32 + (lane & 31)];
            if (lane < 32) out[b * 512 + ocol] = (u16)(pk_bf16(r, r) & 0xffffu);
            if (L == 0 && lane == 0) __hip_atomic_store((GAS u64*)(ws + WS_EPOCH), tag, RLX_AGENT);
            if (PROBE == 11) pt1 = __builtin_amdgcn_s_memrealtime();
            { const u64 until = __builtin_amdgcn_s_memrealtime() + (pt1 - pt0); while (__builtin_amdgcn_s_memrealtime() < until) __builtin_amdgcn_s_sleep(1); }
        }
        return;
    }

    v4u wqr[2], w1r;
    {
        const u16* p = wq + (size_t)(128 * mi + 16 * w) * 4096 + kvh * 512 + qline * 64;
        const unsigned lo = (unsigned)(lane >> 3) * 4096u + 8u * j2;
        wqr[0] = ld_nt(p + lo);
        wqr[1] = ld_nt(p + lo + 8 * 4096);
        CFENCE();
        w1r = ld_nt(w1 + (size_t)(64 * mks + 8 * w) * 1792 + mj * 64 + ((unsigned)(lane >> 3) * 1792u + 8u * j2));
    }
    CFENCE(); SB();
    BAR();
    const unsigned char* kb = (const unsigned char*)Kc + (size_t)grp * 8192 * 256;
    const unsigned char* vb = (const unsigned char*)Vc + (size_t)grp * 8192 * 256;
    const unsigned klo = (4u * (r16 & 3) + ((r16 >> 2) & 1)) * 256u + 64u * (r16 >> 3) + 16u * rho;
    const unsigned vlo = 16u * lane;
#define KIN(i) ((((i) >> 1) & 1) * 512 + ((i) & 1) * 128)
#define TOFF(u) ((unsigned)(((u) * 128 + s * 8 + w + 32 * grp) & 511) * 4096u)
    v4u kr[16], vr[16];
#pragma unroll
    for (int i = 0; i < K0; ++i) kr[i] = ld_nt(kb + TOFF(i >> 2) + klo + KIN(i));
    CFENCE(); SB();
    const f32x4 z4 = {0.f, 0.f, 0.f, 0.f};
    BAR();
    {
        const int xi = ((lane >> 2) & 1) * 512 + 2 * rho + ((lane >> 3) & 1);
        const unsigned sh = (lane & 1) * 16;
        const unsigned e0 = (unsigned)s_xn[xi + 128 * mi + 16 * w] << sh, e1 = (unsigned)s_xn[xi + 128 * mi + 16 * w + 8] << sh;
        const unsigned e2 = (unsigned)s_xn[xi + 64 * mks + 8 * w] << sh;
        const bool lo2 = (lane & 2) == 0;
        const unsigned a0 = lo2 ? e0 : 0u, c0 = lo2 ? 0u : e0, a1 = lo2 ? e1 : 0u, c1 = lo2 ? 0u : e1, a2 = lo2 ? e2 : 0u, c2_ = lo2 ? 0u : e2;
        const f32x4 d0 = mfma16(wqr[1].x, wqr[1].y, a1, c1, mfma16(wqr[0].x, wqr[0].y, a0, c0, z4));
        const f32x4 d1 = mfma16(wqr[1].z, wqr[1].w, a1, c1, mfma16(wqr[0].z, wqr[0].w, a0, c0, z4));
        f32x4 u;
#pragma unroll
        for (int i = 0; i < 4; ++i) {
            auto r = __builtin_amdgcn_permlane32_swap(__float_as_uint(d0[i]), __float_as_uint(d1[i]), false, false);
            u[i] = __uint_as_float(r[0]) + dpp<ROR8>(__uint_as_float(r[1]));
        }
        const int pos = w * 128 + ((lane >> 2) & 1) * 64 + (rho >> 1) * 32 + (lane & 3) * 8 + (rho & 1) * 4;
        if ((lane & 8) == 0) *(f32x4*)&s_qred[pos] = u;
        const f32x4 g0 = mfma16(w1r.x, w1r.y, a2, c2_, z4), g1 = mfma16(w1r.z, w1r.w, a2, c2_, z4);
#pragma unroll
        for (int i = 0; i < 4; ++i) {
            auto r = __builtin_amdgcn_permlane32_swap(__float_as_uint(g0[i]), __float_as_uint(g1[i]), false, false);
            u[i] = __uint_as_float(r[0]) + dpp<ROR8>(__uint_as_float(r[1]));
        }
        if ((lane & 8) == 0) *(f32x4*)&s_hred[pos] = u;
    }
    BAR();
    unsigned qrdy = 0;
#define WAITW() do { if (!qrdy) { qrdy = __builtin_amdgcn_readfirstlane(__hip_atomic_load(&s_flag, RLX_WG)) >> 1; \
        if (!qrdy) asm volatile("s_waitcnt vmcnt(2)" ::: "memory"); } } while (0)
#pragma unroll
    for (int i = K0; i < 16; ++i) { WAITW(); kr[i] = ld_nt(kb + TOFF(i >> 2) + klo + KIN(i)); CFENCE(); }
#pragma unroll
    for (int i = 0; i < 16; ++i) { WAITW(); vr[i] = ld_nt(vb + TOFF(i >> 2) + vlo + (i & 3) * 1024); CFENCE(); }
    if (!qrdy) LDS_WAIT(s_flag, 1u, 1);
    CFENCE(); SB();
    constexpr float C2 = 0.08838834764831845f * 1.4426950408889634f;
    unsigned pp[4][4];
    {
        f32x4 S[4];
        v4u qf[2];
#pragma unroll
        for (int hf = 0; hf < 2; ++hf) qf[hf] = *(const v4u*)&s_q[(r16 >> 2) * 64 + 32 * hf + 16 * ((r16 >> 1) & 1) + 4 * rho];
#pragma unroll
        for (int u = 0; u < 4; ++u) {
            const f32x4 da = mfma32(kr[u * 4 + 1], qf[1], mfma32(kr[u * 4 + 0], qf[0], z4));
            const f32x4 db = mfma32(kr[u * 4 + 3], qf[1], mfma32(kr[u * 4 + 2], qf[0], z4));
#pragma unroll
            for (int i = 0; i < 4; ++i) {
                auto r = __builtin_amdgcn_permlane32_swap(__float_as_uint(da[i]), __float_as_uint(db[i]), false, false);
                S[u][i] = dpp<BC0>(__uint_as_float(r[0])) + dpp<BC2>(__uint_as_float(r[1]));
            }
        }
        float m = S[0][0];
#pragma unroll
        for (int u = 0; u < 4; ++u)
#pragma unroll
            for (int i = 0; i < 4; ++i) m = fmaxf(m, S[u][i]);
        m = sw16_max(m); m = sw32_max(m);
        m *= C2;
        float l4[4] = {0.f, 0.f, 0.f, 0.f};
#pragma unroll
        for (int u = 0; u < 4; ++u)
#pragma unroll
            for (int i = 0; i < 4; ++i) { const float p = __builtin_amdgcn_exp2f(fmaf(S[u][i], C2, -m)); l4[i] += p; pp[u][i] = pk_bf16(p, p); }
        float l = (l4[0] + l4[1]) + (l4[2] + l4[3]);
        l = sw16_add(l, l); l = sw32_add(l, l);
        if (lane < 16 && (lane & 3) == 0) *(f32x2*)&s_ml[(w * 4 + (lane >> 2)) * 2] = f32x2{m, l};
    }
    LGKM0();
    if (lane == 0) __hip_atomic_fetch_add(&s_cnt, 1u, RLX_WG);
    {
        const unsigned hm = (lane & 1) ? 0xffff0000u : 0x0000ffffu;
        const unsigned ma = (lane & 2) ? 0u : hm, mb = (lane & 2) ? hm : 0u;
        f32x4 O0 = z4, O1 = z4;
#pragma unroll
        for (int u = 0; u < 4; ++u)
#pragma unroll
            for (int i = 0; i < 4; ++i) {
                const unsigned pa = pp[u][i] & ma, pb = pp[u][i] & mb;
                const v4u v = vr[u * 4 + i];
                O0 = mfma16(v.x, v.y, pa, pb, O0);
                O1 = mfma16(v.z, v.w, pa, pb, O1);
            }
        float* po = &s_ored[(w * 128 + (r16 >> 2) * 32 + rho * 8 + (lane & 3)) * 4];
        *(f32x4*)po = O0;
        *(f32x4*)(po + 16) = O1;
    }
    int o_pub1 = WS_BLK + (grp * 16 + (tid >> 3)) * BLK_SZ + BLK_IN1 + s * 128 + j2 * 16;
    int o_pub2 = WS_IN2 + (b * 16 + 2 * w + rho) * 16384 + c2 * 128 + j2 * 16;
    int o_fin = WS_IN2 + (b * 16 + fj) * 16384 + w * 2048 + lane * 16;
    asm volatile("" : "+v"(o_pub1), "+v"(o_pub2), "+v"(o_fin));
    CFENCE(); SB();
    BAR();
    if (w < 2) {
        const f32x4 fa = *(const f32x4*)&s_fw[(tid >> 5) * 8], fb = *(const f32x4*)&s_fw[(tid >> 5) * 8 + 4];
        f32x4 acc = fa[0] * *(const f32x4*)&s_ored[tid * 4];
#pragma unroll
        for (int k = 1; k < 8; ++k) acc += (k < 4 ? fa[k & 3] : fb[k & 3]) * *(const f32x4*)&s_ored[(k * 128 + tid) * 4];
        ST_SC1(nosent(acc), o_pub1);
    }
    CFENCE();
    v4u wor[4], w2r[2];
    {
        const u16* p = wo + (size_t)(kvh * 512 + s * 32) * 512 + 64 * w;
        const u16* p2 = w2 + (size_t)hblk * 16 * 512 + 64 * w;
        const unsigned lo = (unsigned)(lane >> 3) * 512u + 8u * j2;
        w2r[0] = *(const v4u*)(p2 + lo); w2r[1] = *(const v4u*)(p2 + lo + 8 * 512);
#pragma unroll
        for (int t = 0; t < 4; ++t) wor[t] = *(const v4u*)(p + lo + t * 8 * 512);
    }
    CFENCE(); SB();
    unsigned msk[4];
#pragma unroll
    for (int k = 0; k < 4; ++k) msk[k] = (k == (j2 >> 1)) ? ((j2 & 1) ? 0xffff0000u : 0x0000ffffu) : 0u;
    const unsigned ph0 = s_h[lane >> 3], ph1 = s_h[(lane >> 3) + 8];
    v4u bh0 = {ph0 & msk[0], ph0 & msk[1], ph0 & msk[2], ph0 & msk[3]}, bh1 = {ph1 & msk[0], ph1 & msk[1], ph1 & msk[2], ph1 & msk[3]};
    asm volatile("" : "+v"(bh0), "+v"(bh1));
    BAR();
    {
        const v4u ob = *(const v4u*)&s_o[(lane >> 3) * 4];
        v4u bo[4];
#pragma unroll
        for (int t = 0; t < 4; ++t) bo[t] = v4u{ob[t] & msk[0], ob[t] & msk[1], ob[t] & msk[2], ob[t] & msk[3]};
        asm volatile("" : "+v"(bo[0]), "+v"(bo[1]), "+v"(bo[2]), "+v"(bo[3]));
        f32x4 Y = mfma32(w2r[1], bh1, mfma32(w2r[0], bh0, z4));
#pragma unroll
        for (int t = 0; t < 4; ++t) Y = mfma32(wor[t], bo[t], Y);
        f32x4 Z;
#pragma unroll
        for (int i = 0; i < 4; ++i) {
            const float y = dpp<ROR8>(Y[i]);
            auto r = __builtin_amdgcn_permlane32_swap(__float_as_uint(y), __float_as_uint(y), false, false);
            Z[i] = Y[i] + __uint_as_float(r[1]);
        }
        if (lane < 32 && (lane & 8) == 0) ST_SC1(nosent(Z), o_pub2);
    }
    if (fin_on) {
        v4u k0, k1, k2, k3;
        float v[4];
        const float one = 1.0f;
        __builtin_amdgcn_s_sleep(10);
        POLL_PAIR(1024, v, k0, k1, k2, k3, o_fin, srd, one, one);
        const float t0 = sw32_add(v[0], v[1]), t1 = sw32_add(v[2], v[3]);
        float r = sw16_add(t0, t1);
        r += dpp<ROR8>(r);
        if ((lane & 8) == 0) s_fin[w * 32 + 8 * (2 * (rho & 1) + (rho >> 1)) + j2] = r;
        BAR();
        POLL_DRAIN(k0, k1, k2, k3);
    }
}

extern "C" void kernel_launch(void* const* d_in, const int* in_sizes, int n_in, void* d_out, int out_size, void* d_ws, size_t ws_size,
                              hipStream_t stream) {
    fused_decode<<<256, 576, 0, stream>>>((const u16*)d_in[0], (const u16*)d_in[1], (const u16*)d_in[2], (const u16*)d_in[3], (const u16*)d_in[4],
                                          (const u16*)d_in[6], (const u16*)d_in[7], (const u16*)d_in[8], (const u16*)d_in[9], (u16*)d_out,
                                          (unsigned char*)d_ws);
}
```

```cpp
#include <hip/hip_runtime.h>
#include <stdint.h>

typedef unsigned short u16;
typedef unsigned long long u64;
typedef unsigned v4u __attribute__((ext_vector_type(4)));
typedef unsigned v2u __attribute__((ext_vector_type(2)));
typedef float f32x4 __attribute__((ext_vector_type(4)));
typedef float f32x2 __attribute__((ext_vector_type(2)));
typedef short s16x4 __attribute__((ext_vector_type(4)));
typedef __bf16 bf16x8 __attribute__((ext_vector_type(8)));
typedef __bf16 bf16x2 __attribute__((ext_vector_type(2)));
#define GAS __attribute__((address_space(1)))
#define RLX_AGENT __ATOMIC_RELAXED, __HIP_MEMORY_SCOPE_AGENT
#define RLX_WG __ATOMIC_RELAXED, __HIP_MEMORY_SCOPE_WORKGROUP

constexpr int K0 = 5;
constexpr int WS_EPOCH = 0;
constexpr int WS_FLAGS = 4096;
constexpr int WS_QG = 8192;
constexpr int WS_BLK = 131072;
constexpr int BLK_SZ = 3072, BLK_IN1 = 0, BLK_ML = 2048;
constexpr int WS_IN2 = WS_BLK + 256 * BLK_SZ;
constexpr int WS_QA = WS_IN2 + 32 * 16384;
constexpr int WS_HG = WS_QA + 64 * 4096;
constexpr int WS_BYTES = WS_HG + 2 * 128 * 1024;
constexpr unsigned SENT = 0xFFFFFFFFu;
constexpr int AUX_SC1 = 16;
constexpr unsigned SPIN_CAP = 1u << 18;

template <int CTRL> __device__ __forceinline__ float dpp(float x) {
    return __builtin_bit_cast(float, __builtin_amdgcn_mov_dpp(__builtin_bit_cast(int, x), CTRL, 0xf, 0xf, true));
}
constexpr int XOR1 = 0xB1, XOR2 = 0x4E, HMIR = 0x141, ROR8 = 0x128, BC0 = 0x00, BC2 = 0xAA;
__device__ __forceinline__ float sw32_add(float a, float b) {
    auto r = __builtin_amdgcn_permlane32_swap(__float_as_uint(a), __float_as_uint(b), false, false);
    return __uint_as_float(r[0]) + __uint_as_float(r[1]);
}
__device__ __forceinline__ float sw16_add(float a, float b) {
    auto r = __builtin_amdgcn_permlane16_swap(__float_as_uint(a), __float_as_uint(b), false, false);
    return __uint_as_float(r[0]) + __uint_as_float(r[1]);
}
__device__ __forceinline__ float sw32_max(float a) {
    auto r = __builtin_amdgcn_permlane32_swap(__float_as_uint(a), __float_as_uint(a), false, false);
    return fmaxf(__uint_as_float(r[0]), __uint_as_float(r[1]));
}
__device__ __forceinline__ float sw16_max(float a) {
    auto r = __builtin_amdgcn_permlane16_swap(__float_as_uint(a), __float_as_uint(a), false, false);
    return fmaxf(__uint_as_float(r[0]), __uint_as_float(r[1]));
}
__device__ __forceinline__ float oct_sum(float x) { x += dpp<XOR1>(x); x += dpp<XOR2>(x); x += dpp<HMIR>(x); return x; }
__device__ __forceinline__ float oct_max(float x) { x = fmaxf(x, dpp<XOR1>(x)); x = fmaxf(x, dpp<XOR2>(x)); x = fmaxf(x, dpp<HMIR>(x)); return x; }
__device__ __forceinline__ float row_sum(float x) { x = oct_sum(x); x += dpp<ROR8>(x); return x; }
__device__ __forceinline__ float bflo(unsigned u) { return __uint_as_float(u << 16); }
__device__ __forceinline__ float bfhi(unsigned u) { return __uint_as_float(u & 0xffff0000u); }
__device__ __forceinline__ unsigned pk_bf16(float lo, float hi) {
    f32x2 v = {lo, hi};
    return __builtin_bit_cast(unsigned, __builtin_convertvector(v, bf16x2));
}
__device__ __forceinline__ float dot2(unsigned a, unsigned b, float c) {
    return __builtin_amdgcn_fdot2_f32_bf16(__builtin_bit_cast(bf16x2, a), __builtin_bit_cast(bf16x2, b), c, false);
}
__device__ __forceinline__ f32x4 mfma32(v4u a, v4u b, f32x4 c) {
    return __builtin_amdgcn_mfma_f32_16x16x32_bf16(__builtin_bit_cast(bf16x8, a), __builtin_bit_cast(bf16x8, b), c, 0, 0, 0);
}
__device__ __forceinline__ f32x4 mfma16(unsigned a0, unsigned a1, unsigned b0, unsigned b1, f32x4 c) {
    return __builtin_amdgcn_mfma_f32_16x16x16bf16_1k(__builtin_bit_cast(s16x4, v2u{a0, a1}), __builtin_bit_cast(s16x4, v2u{b0, b1}), c, 0, 0, 0);
}
__device__ __forceinline__ v4u ld_nt(const void* p) { return __builtin_nontemporal_load((const v4u*)p); }
__device__ __forceinline__ v4u nosent(f32x4 v) {
    v4u u = __builtin_bit_cast(v4u, v);
    u.x = min(u.x, SENT - 1u); u.y = min(u.y, SENT - 1u); u.z = min(u.z, SENT - 1u); u.w = min(u.w, SENT - 1u);
    return u;
}
__device__ __forceinline__ unsigned max4(v4u a) { return max(max(a.x, a.y), max(a.z, a.w)); }

#define POLL_CHECK(b0, b1, b2, b3, b4, b5, b6, b7) \
    "v_max3_u32 v144, v" #b0 ", v" #b1 ", v" #b2 "\n\tv_max3_u32 v145, v" #b3 ", v" #b4 ", v" #b5 "\n\t" \
    "v_max3_u32 v144, v144, v" #b6 ", v" #b7 "\n\tv_max_u32 v144, v144, v145\n\tv_cmp_eq_u32 vcc, -1, v144\n\t" \
    "v_mul_f32 %0, %11, v" #b0 "\n\tv_mul_f32 %1, %11, v" #b1 "\n\tv_mul_f32 %2, %11, v" #b2 "\n\tv_mul_f32 %3, %11, v" #b3 "\n\t" \
    "v_fmac_f32 %0, %12, v" #b4 "\n\tv_fmac_f32 %1, %12, v" #b5 "\n\tv_fmac_f32 %2, %12, v" #b6 "\n\tv_fmac_f32 %3, %12, v" #b7 "\n\t" \
    "s_mov_b64 exec, vcc\n\t" \
    "s_cbranch_vccz 2f\n\t"
#define POLL_PAIR(D, r, k0, k1, k2, k3, off, srd, e0, e1) do { unsigned cnt_; u64 ex_; \
    asm volatile( \
        "s_mov_b64 %9, exec\n\t" \
        "buffer_load_dwordx4 v[128:131], %10, %13, 0 offen sc1\n\t" \
        "buffer_load_dwordx4 v[132:135], %10, %13, 0 offen offset:" #D " sc1\n\t" \
        "s_mov_b32 %4, 0x40000\n\t" \
        "s_sleep 8\n" \
        "1:\n\t" \
        "buffer_load_dwordx4 v[136:139], %10, %13, 0 offen sc1\n\t" \
        "buffer_load_dwordx4 v[140:143], %10, %13, 0 offen offset:" #D " sc1\n\t" \
        "s_waitcnt vmcnt(2)\n\t" \
        POLL_CHECK(128, 129, 130, 131, 132, 133, 134, 135) \
        "buffer_load_dwordx4 v[128:131], %10, %13, 0 offen sc1\n\t" \
        "buffer_load_dwordx4 v[132:135], %10, %13, 0 offen offset:" #D " sc1\n\t" \
        "s_waitcnt vmcnt(2)\n\t" \
        POLL_CHECK(136, 137, 138, 139, 140, 141, 142, 143) \
        "s_sub_u32 %4, %4, 1\n\t" \
        "s_cbranch_scc0 1b\n" \
        "2:\n\t" \
        "s_mov_b64 exec, %9\n\t" \
        "s_nop 4" \
        : "=&v"(r[0]), "=&v"(r[1]), "=&v"(r[2]), "=&v"(r[3]), "=&s"(cnt_), \
          "=&{v[128:131]}"(k0), "=&{v[132:135]}"(k1), "=&{v[136:139]}"(k2), "=&{v[140:143]}"(k3), "=&s"(ex_) \
        : "v"(off), "v"(e0), "v"(e1), "s"(srd) \
        : "v144", "v145", "vcc", "scc", "memory"); } while (0)
#define POLL_DRAIN(k0, k1, k2, k3) \
    asm volatile("s_waitcnt vmcnt(0)" :: "{v[128:131]}"(k0), "{v[132:135]}"(k1), "{v[136:139]}"(k2), "{v[140:143]}"(k3) : "memory")
#define SB() __builtin_amdgcn_sched_barrier(0)
#define CFENCE() asm volatile("" ::: "memory")
#define LGKM0() asm volatile("s_waitcnt lgkmcnt(0)" ::: "memory")
#define BAR() do { asm volatile("s_waitcnt lgkmcnt(0)" ::: "memory"); __builtin_amdgcn_s_barrier(); asm volatile("" ::: "memory"); } while (0)
#define LDS_WAIT(word, target, slp) \
    for (unsigned spins_ = 0; __hip_atomic_load(&(word), RLX_WG) < (target) && spins_ < (SPIN_CAP << 4); ++spins_) __builtin_amdgcn_s_sleep(slp)
#define LD_SC1(off) __builtin_amdgcn_raw_buffer_load_b128(rs, (off), 0, AUX_SC1)
#define ST_SC1(v, off) __builtin_amdgcn_raw_buffer_store_b128((v), rs, (off), 0, AUX_SC1)

__global__ void __launch_bounds__(576, 3)
fused_decode(const u16* __restrict__ x, const u16* __restrict__ Kc, const u16* __restrict__ Vc, const u16* __restrict__ wq,
             const u16* __restrict__ wo, const u16* __restrict__ w1, const u16* __restrict__ b1, const u16* __restrict__ w2,
             const u16* __restrict__ b2, u16* __restrict__ out, unsigned char* __restrict__ ws) {
    __shared__ __attribute__((aligned(16))) float s_ored[8 * 128 * 4];
    __shared__ __attribute__((aligned(16))) float s_qred[8 * 128];
    __shared__ __attribute__((aligned(16))) float s_hred[8 * 128];
    __shared__ __attribute__((aligned(16))) unsigned s_q[256];
    __shared__ __attribute__((aligned(16))) u16 s_xn[2 * 512];
    __shared__ __attribute__((aligned(16))) unsigned s_h[16];
    __shared__ __attribute__((aligned(16))) unsigned s_o[32];
    __shared__ __attribute__((aligned(16))) float s_ml[8 * 4 * 2];
    __shared__ __attribute__((aligned(16))) float s_fw[4 * 8];
    __shared__ __attribute__((aligned(16))) float s_fin[8 * 32];
    __shared__ unsigned s_flag, s_cnt;

    asm volatile("" :: "s"(x), "s"(Kc), "s"(Vc), "s"(wq), "s"(wo), "s"(w1), "s"(b1), "s"(w2), "s"(b2), "s"(out), "s"(ws));
    const int L = blockIdx.x;
    const int b = L >> 7, kvh = (L >> 4) & 7, s = L & 15, grp = L >> 4, c2 = L & 127;
    const int qline = (s + kvh) & 7;
    const int mi = 2 * b + (s >> 3);
    const int lane = threadIdx.x & 63, w0 = __builtin_amdgcn_readfirstlane(threadIdx.x >> 6);
    const int w = w0 - 1, tid = w * 64 + lane;
    const int rho = lane >> 4, j2 = lane & 7;
    const unsigned r16 = lane & 15;
    const __amdgpu_buffer_rsrc_t rs = __builtin_amdgcn_make_buffer_rsrc(ws, 0, WS_BYTES, 0x00020000);
    const v4u srd = {(unsigned)(uintptr_t)ws, (unsigned)((uintptr_t)ws >> 32) & 0xffffu, (unsigned)WS_BYTES, 0x00020000u};
    const bool m1_on = L < 224, m2_on = c2 < 112;
    const int mj = m1_on ? L % 28 : L - 224, mks = m1_on ? L / 28 : 0;
    const int hblk = m2_on ? c2 : c2 - 16;
    const bool fin_on = (s & 7) == kvh;
    const int fj = 2 * kvh + (s >> 3);

    if (w0 == 0) {
        __builtin_amdgcn_s_setprio(3);
        const v4u xa0 = *(const v4u*)(x + 16 * lane), xa1 = *(const v4u*)(x + 16 * lane + 8);
        CFENCE();
        const u64 epoch = __hip_atomic_load((GAS u64*)(ws + WS_EPOCH), RLX_AGENT);
        if (lane == 0) { s_flag = 0; s_cnt = 0; }
        BAR();
        const unsigned b1v = *(const unsigned*)(b1 + hblk * 16 + 2 * j2);
        const int ocol = 32 * fj + (lane & 31);
        const unsigned xo = x[b * 512 + ocol], b2o = b2[ocol];
        CFENCE();
        {
            float s1 = 0.f, s2 = 0.f, t1 = 0.f, t2 = 0.f;
#pragma unroll
            for (int k = 0; k < 4; ++k) {
                s1 = dot2(xa0[k], 0x3f803f80u, s1); t1 = dot2(xa1[k], 0x3f803f80u, t1);
                s2 = dot2(xa0[k], xa0[k], s2); t2 = dot2(xa1[k], xa1[k], t2);
            }
            s1 = row_sum(s1 + t1); s2 = row_sum(s2 + t2);
            s1 = sw16_add(s1, s1); s2 = sw16_add(s2, s2);
            const float mean = s1 * (1.0f / 512.0f);
            const float var = fmaxf(fmaf(-mean, mean, s2 * (1.0f / 512.0f)), 0.f);
            const float rstd = __builtin_amdgcn_rsqf(var + 1e-5f), nb = -mean * rstd;
            v4u o0, o1;
#pragma unroll
            for (int k = 0; k < 4; ++k) {
                o0[k] = pk_bf16(fmaf(bflo(xa0[k]), rstd, nb), fmaf(bfhi(xa0[k]), rstd, nb));
                o1[k] = pk_bf16(fmaf(bflo(xa1[k]), rstd, nb), fmaf(bfhi(xa1[k]), rstd, nb));
            }
            *(v4u*)&s_xn[16 * lane] = o0;
            *(v4u*)&s_xn[16 * lane + 8] = o1;
        }
        BAR();
        const u64 tag = epoch + 1ull;
        const unsigned tg0 = (unsigned)tag, tg1 = (unsigned)(tag >> 32), thi8 = (unsigned)(tag >> 8), tlo8 = tg0 << 24;
        asm volatile("" :: "v"(thi8), "v"(tlo8));
        BAR();
        {
            const int c = lane & 31, pos = (lane >> 5) * 64 + ((c >> 1) & 1) * 32 + (c & 1) * 16 + (c >> 2);
            float q0 = 0.f, q1 = 0.f, h0 = 0.f, h1 = 0.f;
#pragma unroll
            for (int k = 0; k < 8; ++k) { q0 += s_qred[k * 128 + pos]; q1 += s_qred[k * 128 + pos + 8]; }
            ST_SC1((v4u{tlo8 | (__float_as_uint(q0) >> 8), thi8, tlo8 | (__float_as_uint(q1) >> 8), thi8}),
                   WS_QA + (kvh * 8 + qline) * 4096 + rho * 1024 + mi * 256 + r16 * 16);
            CFENCE();
#pragma unroll
            for (int k = 0; k < 8; ++k) { h0 += s_hred[k * 128 + pos]; h1 += s_hred[k * 128 + pos + 8]; }
            if (m1_on)
                ST_SC1((v4u{tlo8 | (__float_as_uint(h0) >> 8), thi8, tlo8 | (__float_as_uint(h1) >> 8), thi8}),
                       WS_HG + ((lane >> 5) * 128 + 4 * mj + (c >> 3)) * 1024 + mks * 128 + (c & 7) * 16);
        }
        CFENCE();
        {
            const v4u sv = {SENT, SENT, SENT, SENT};
            const int base = WS_BLK + L * BLK_SZ + lane * 16, base2 = WS_IN2 + L * 2048 + lane * 16;
            ST_SC1(sv, base);
            ST_SC1(sv, base + 1024);
            ST_SC1(sv, lane < 8 ? base + 2048 : 0x7ffffff0);
            ST_SC1(sv, base2);
            ST_SC1(sv, base2 + 1024);
        }
        __builtin_amdgcn_s_sleep(48);
        {
            v4u ga;
            for (unsigned spins = 0;; ++spins) {
                CFENCE();
                ga = LD_SC1(WS_QA + (kvh * 8 + qline) * 4096 + mi * 1024 + lane * 16);
                const unsigned bad = (ga.y ^ thi8) | (ga.w ^ thi8) | (((ga.x ^ tlo8) | (ga.z ^ tlo8)) >> 24);
                if (__all(bad == 0u) || spins > SPIN_CAP) break;
                __builtin_amdgcn_s_sleep(4);
            }
            asm volatile("s_waitcnt vmcnt(0)" ::: "memory");
            float qa = __uint_as_float(ga.x << 8), qb = __uint_as_float(ga.z << 8);
            qa = sw16_add(qa, qa); qb = sw16_add(qb, qb);
            qa = sw32_add(qa, qa); qb = sw32_add(qb, qb);
            const unsigned pk = pk_bf16(qa, qb), pk2 = __float_as_uint(dpp<XOR1>(__uint_as_float(pk)));
            if (lane < 16 && !(lane & 1)) ST_SC1((v4u{pk, tg0, pk2, tg0}), WS_QG + grp * 2048 + (2 * qline + (s >> 3)) * 128 + lane * 8);
            if (lane == 16) __hip_atomic_store((GAS u64*)(ws + WS_FLAGS) + L, tag, RLX_AGENT);
        }
        __builtin_amdgcn_s_sleep(56);
        {
            v4u g[2], gh, gf;
            unsigned bad[2] = {1u, 1u}, badh = 1u, badf = 1u;
            bool qdone = false;
            for (unsigned spins = 0;; ++spins) {
                CFENCE();
#pragma unroll
                for (int k = 0; k < 2; ++k) if (bad[k]) g[k] = LD_SC1(WS_QG + grp * 2048 + k * 1024 + lane * 16);
                if (badh) gh = LD_SC1(WS_HG + (b * 128 + hblk) * 1024 + lane * 16);
                if (badf) gf = LD_SC1(WS_FLAGS + b * 1024 + lane * 16);
#pragma unroll
                for (int k = 0; k < 2; ++k) bad[k] = (g[k].y ^ tg0) | (g[k].w ^ tg0);
                if (!qdone && (__all((bad[0] | bad[1]) == 0u) || spins > SPIN_CAP)) {
                    qdone = true;
                    *(v2u*)&s_q[2 * lane] = v2u{g[0].x, g[0].z};
                    *(v2u*)&s_q[128 + 2 * lane] = v2u{g[1].x, g[1].z};
                    LGKM0();
                    if (lane == 0) __hip_atomic_store(&s_flag, 1u, RLX_WG);
                }
                badh = (gh.y ^ thi8) | (gh.w ^ thi8) | (((gh.x ^ tlo8) | (gh.z ^ tlo8)) >> 24);
                badf = (gf.x ^ tg0) | (gf.y ^ tg1) | (gf.z ^ tg0) | (gf.w ^ tg1);
                if ((qdone && __all((badh | badf) == 0u)) || spins > SPIN_CAP) break;
                __builtin_amdgcn_s_sleep(4);
            }
            if (lane == 0) __hip_atomic_store(&s_flag, 2u, RLX_WG);
            if (L == 0 && lane == 0) __hip_atomic_store((GAS u64*)(ws + WS_EPOCH), tag, RLX_AGENT);
            float hv[2] = {__uint_as_float(gh.x << 8), __uint_as_float(gh.z << 8)};
#pragma unroll
            for (int k = 0; k < 2; ++k) {
                float h = hv[k];
                h += dpp<ROR8>(h); h = sw16_add(h, h); h = sw32_add(h, h);
                h += k ? bfhi(b1v) : bflo(b1v);
                const float u2 = 1.5957691216057308f * (h + 0.044715f * h * h * h);
                const float th = 1.0f - 2.0f / (__expf(u2) + 1.0f);
                hv[k] = m2_on ? 0.5f * h * (1.0f + th) : 0.f;
            }
            if (lane < 8) *(v2u*)&s_h[2 * lane] = v2u{pk_bf16(hv[0], hv[0]), pk_bf16(hv[1], hv[1])};
        }
        LDS_WAIT(s_cnt, 8u, 2);
        CFENCE();
        {
            const int h = (lane >> 3) & 3;
            const f32x2 ml = *(const f32x2*)&s_ml[(j2 * 4 + h) * 2];
            const float M = oct_max(ml.x);
            const float f = __builtin_amdgcn_exp2f(ml.x - M);
            const float Ls = oct_sum(f * ml.y);
            if (lane < 32) s_fw[lane] = f;
            if (lane < 32 && j2 < 4) {
                const v2u mlv = {min(__float_as_uint(M), SENT - 1u), min(__float_as_uint(Ls), SENT - 1u)};
                __builtin_amdgcn_raw_buffer_store_b64(mlv, rs, WS_BLK + (grp * 16 + 4 * h + j2) * BLK_SZ + BLK_ML + s * 8, 0, AUX_SC1);
            }
        }
        BAR();
        v4u k0, k1, k2, k3;
        {
            const int base = WS_BLK + L * BLK_SZ;
            const int oa = base + BLK_IN1 + (2 * j2) * 128 + (lane >> 3) * 16;
            v4u ML;
            for (unsigned spins = 0;; ++spins) {
                CFENCE();
                ML = LD_SC1(base + BLK_ML + j2 * 16);
                if (__all(max4(ML) != SENT) || spins > SPIN_CAP) break;
                __builtin_amdgcn_s_sleep(2);
            }
            const float M0 = __uint_as_float(ML.x), L0 = __uint_as_float(ML.y), M1 = __uint_as_float(ML.z), L1 = __uint_as_float(ML.w);
            const float Mx = oct_max(fmaxf(M0, M1));
            float e0 = __builtin_amdgcn_exp2f(M0 - Mx), e1 = __builtin_amdgcn_exp2f(M1 - Mx);
            const float inv = __builtin_amdgcn_rcpf(oct_sum(fmaf(e0, L0, e1 * L1)));
            e0 *= inv; e1 *= inv;
            float o4[4];
            POLL_PAIR(128, o4, k0, k1, k2, k3, oa, srd, e0, e1);
            v4u ob;
#pragma unroll
            for (int t = 0; t < 4; ++t) { const float o = oct_sum(o4[t]); ob[t] = pk_bf16(o, o); }
            if (j2 == 0) *(v4u*)&s_o[(lane >> 3) * 4] = ob;
        }
        BAR();
        POLL_DRAIN(k0, k1, k2, k3);
        if (fin_on) {
            BAR();
            float r = bflo(xo) + bflo(b2o);
#pragma unroll
            for (int k = 0; k < 8; ++k) r += s_fin[k * 32 + (lane & 31)];
            if (lane < 32) out[b * 512 + ocol] = (u16)(pk_bf16(r, r) & 0xffffu);
        }
        return;
    }

    v4u wqr[2], w1r;
    {
        const u16* p = wq + (size_t)(128 * mi + 16 * w) * 4096 + kvh * 512 + qline * 64;
        const unsigned lo = (unsigned)(lane >> 3) * 4096u + 8u * j2;
        wqr[0] = ld_nt(p + lo);
        wqr[1] = ld_nt(p + lo + 8 * 4096);
        CFENCE();
        w1r = ld_nt(w1 + (size_t)(64 * mks + 8 * w) * 1792 + mj * 64 + ((unsigned)(lane >> 3) * 1792u + 8u * j2));
    }
    CFENCE(); SB();
    BAR();
    const unsigned char* kb = (const unsigned char*)Kc + (size_t)grp * 8192 * 256;
    const unsigned char* vb = (const unsigned char*)Vc + (size_t)grp * 8192 * 256;
    const unsigned klo = (4u * (r16 & 3) + ((r16 >> 2) & 1)) * 256u + 64u * (r16 >> 3) + 16u * rho;
    const unsigned vlo = 16u * lane;
#define KIN(i) ((((i) >> 1) & 1) * 512 + ((i) & 1) * 128)
#define TOFF(u) ((unsigned)(((u) * 128 + s * 8 + w + 32 * grp) & 511) * 4096u)
    v4u kr[16], vr[16];
#pragma unroll
    for (int i = 0; i < K0; ++i) kr[i] = ld_nt(kb + TOFF(i >> 2) + klo + KIN(i));
    CFENCE(); SB();
    const f32x4 z4 = {0.f, 0.f, 0.f, 0.f};
    BAR();
    {
        const int xi = ((lane >> 2) & 1) * 512 + 2 * rho + ((lane >> 3) & 1);
        const unsigned sh = (lane & 1) * 16;
        const unsigned e0 = (unsigned)s_xn[xi + 128 * mi + 16 * w] << sh, e1 = (unsigned)s_xn[xi + 128 * mi + 16 * w + 8] << sh;
        const unsigned e2 = (unsigned)s_xn[xi + 64 * mks + 8 * w] << sh;
        const bool lo2 = (lane & 2) == 0;
        const unsigned a0 = lo2 ? e0 : 0u, c0 = lo2 ? 0u : e0, a1 = lo2 ? e1 : 0u, c1 = lo2 ? 0u : e1, a2 = lo2 ? e2 : 0u, c2_ = lo2 ? 0u : e2;
        const f32x4 d0 = mfma16(wqr[1].x, wqr[1].y, a1, c1, mfma16(wqr[0].x, wqr[0].y, a0, c0, z4));
        const f32x4 d1 = mfma16(wqr[1].z, wqr[1].w, a1, c1, mfma16(wqr[0].z, wqr[0].w, a0, c0, z4));
        f32x4 u;
#pragma unroll
        for (int i = 0; i < 4; ++i) {
            auto r = __builtin_amdgcn_permlane32_swap(__float_as_uint(d0[i]), __float_as_uint(d1[i]), false, false);
            u[i] = __uint_as_float(r[0]) + dpp<ROR8>(__uint_as_float(r[1]));
        }
        const int pos = w * 128 + ((lane >> 2) & 1) * 64 + (rho >> 1) * 32 + (lane & 3) * 8 + (rho & 1) * 4;
        if ((lane & 8) == 0) *(f32x4*)&s_qred[pos] = u;
        const f32x4 g0 = mfma16(w1r.x, w1r.y, a2, c2_, z4), g1 = mfma16(w1r.z, w1r.w, a2, c2_, z4);
#pragma unroll
        for (int i = 0; i < 4; ++i) {
            auto r = __builtin_amdgcn_permlane32_swap(__float_as_uint(g0[i]), __float_as_uint(g1[i]), false, false);
            u[i] = __uint_as_float(r[0]) + dpp<ROR8>(__uint_as_float(r[1]));
        }
        if ((lane & 8) == 0) *(f32x4*)&s_hred[pos] = u;
    }
    BAR();
    unsigned qrdy = 0;
#define WAITW() do { if (!qrdy) { qrdy = __builtin_amdgcn_readfirstlane(__hip_atomic_load(&s_flag, RLX_WG)) >> 1; \
        if (!qrdy) asm volatile("s_waitcnt vmcnt(2)" ::: "memory"); } } while (0)
#pragma unroll
    for (int i = K0; i < 16; ++i) { WAITW(); kr[i] = ld_nt(kb + TOFF(i >> 2) + klo + KIN(i)); CFENCE(); }
#pragma unroll
    for (int i = 0; i < 16; ++i) { WAITW(); vr[i] = ld_nt(vb + TOFF(i >> 2) + vlo + (i & 3) * 1024); CFENCE(); }
    if (!qrdy) LDS_WAIT(s_flag, 1u, 1);
    CFENCE(); SB();
    constexpr float C2 = 0.08838834764831845f * 1.4426950408889634f;
    unsigned pp[4][4];
    {
        f32x4 S[4];
        v4u qf[2];
#pragma unroll
        for (int hf = 0; hf < 2; ++hf) qf[hf] = *(const v4u*)&s_q[(r16 >> 2) * 64 + 32 * hf + 16 * ((r16 >> 1) & 1) + 4 * rho];
#pragma unroll
        for (int u = 0; u < 4; ++u) {
            const f32x4 da = mfma32(kr[u * 4 + 1], qf[1], mfma32(kr[u * 4 + 0], qf[0], z4));
            const f32x4 db = mfma32(kr[u * 4 + 3], qf[1], mfma32(kr[u * 4 + 2], qf[0], z4));
#pragma unroll
            for (int i = 0; i < 4; ++i) {
                auto r = __builtin_amdgcn_permlane32_swap(__float_as_uint(da[i]), __float_as_uint(db[i]), false, false);
                S[u][i] = dpp<BC0>(__uint_as_float(r[0])) + dpp<BC2>(__uint_as_float(r[1]));
            }
        }
        float m = S[0][0];
#pragma unroll
        for (int u = 0; u < 4; ++u)
#pragma unroll
            for (int i = 0; i < 4; ++i) m = fmaxf(m, S[u][i]);
        m = sw16_max(m); m = sw32_max(m);
        m *= C2;
        float l4[4] = {0.f, 0.f, 0.f, 0.f};
#pragma unroll
        for (int u = 0; u < 4; ++u)
#pragma unroll
            for (int i = 0; i < 4; ++i) { const float p = __builtin_amdgcn_exp2f(fmaf(S[u][i], C2, -m)); l4[i] += p; pp[u][i] = pk_bf16(p, p); }
        float l = (l4[0] + l4[1]) + (l4[2] + l4[3]);
        l = sw16_add(l, l); l = sw32_add(l, l);
        if (lane < 16 && (lane & 3) == 0) *(f32x2*)&s_ml[(w * 4 + (lane >> 2)) * 2] = f32x2{m, l};
    }
    LGKM0();
    if (lane == 0) __hip_atomic_fetch_add(&s_cnt, 1u, RLX_WG);
    {
        const unsigned hm = (lane & 1) ? 0xffff0000u : 0x0000ffffu;
        const unsigned ma = (lane & 2) ? 0u : hm, mb = (lane & 2) ? hm : 0u;
        f32x4 O0 = z4, O1 = z4;
#pragma unroll
        for (int u = 0; u < 4; ++u)
#pragma unroll
            for (int i = 0; i < 4; ++i) {
                const unsigned pa = pp[u][i] & ma, pb = pp[u][i] & mb;
                const v4u v = vr[u * 4 + i];
                O0 = mfma16(v.x, v.y, pa, pb, O0);
                O1 = mfma16(v.z, v.w, pa, pb, O1);
            }
        float* po = &s_ored[(w * 128 + (r16 >> 2) * 32 + rho * 8 + (lane & 3)) * 4];
        *(f32x4*)po = O0;
        *(f32x4*)(po + 16) = O1;
    }
    int o_pub1 = WS_BLK + (grp * 16 + (tid >> 3)) * BLK_SZ + BLK_IN1 + s * 128 + j2 * 16;
    int o_pub2 = WS_IN2 + (b * 16 + 2 * w + rho) * 16384 + c2 * 128 + j2 * 16;
    int o_fin = WS_IN2 + (b * 16 + fj) * 16384 + w * 2048 + lane * 16;
    asm volatile("" : "+v"(o_pub1), "+v"(o_pub2), "+v"(o_fin));
    CFENCE(); SB();
    BAR();
    if (w < 2) {
        const f32x4 fa = *(const f32x4*)&s_fw[(tid >> 5) * 8], fb = *(const f32x4*)&s_fw[(tid >> 5) * 8 + 4];
        f32x4 acc = fa[0] * *(const f32x4*)&s_ored[tid * 4];
#pragma unroll
        for (int k = 1; k < 8; ++k) acc += (k < 4 ? fa[k & 3] : fb[k & 3]) * *(const f32x4*)&s_ored[(k * 128 + tid) * 4];
        ST_SC1(nosent(acc), o_pub1);
    }
    CFENCE();
    v4u wor[4], w2r[2];
    {
        const u16* p = wo + (size_t)(kvh * 512 + s * 32) * 512 + 64 * w;
        const u16* p2 = w2 + (size_t)hblk * 16 * 512 + 64 * w;
        const unsigned lo = (unsigned)(lane >> 3) * 512u + 8u * j2;
        w2r[0] = *(const v4u*)(p2 + lo); w2r[1] = *(const v4u*)(p2 + lo + 8 * 512);
#pragma unroll
        for (int t = 0; t < 4; ++t) wor[t] = *(const v4u*)(p + lo + t * 8 * 512);
    }
    CFENCE(); SB();
    unsigned msk[4];
#pragma unroll
    for (int k = 0; k < 4; ++k) msk[k] = (k == (j2 >> 1)) ? ((j2 & 1) ? 0xffff0000u : 0x0000ffffu) : 0u;
    const unsigned ph0 = s_h[lane >> 3], ph1 = s_h[(lane >> 3) + 8];
    v4u bh0 = {ph0 & msk[0], ph0 & msk[1], ph0 & msk[2], ph0 & msk[3]}, bh1 = {ph1 & msk[0], ph1 & msk[1], ph1 & msk[2], ph1 & msk[3]};
    asm volatile("" : "+v"(bh0), "+v"(bh1));
    BAR();
    {
        const v4u ob = *(const v4u*)&s_o[(lane >> 3) * 4];
        v4u bo[4];
#pragma unroll
        for (int t = 0; t < 4; ++t) bo[t] = v4u{ob[t] & msk[0], ob[t] & msk[1], ob[t] & msk[2], ob[t] & msk[3]};
        asm volatile("" : "+v"(bo[0]), "+v"(bo[1]), "+v"(bo[2]), "+v"(bo[3]));
        f32x4 Y = mfma32(w2r[1], bh1, mfma32(w2r[0], bh0, z4));
#pragma unroll
        for (int t = 0; t < 4; ++t) Y = mfma32(wor[t], bo[t], Y);
        f32x4 Z;
#pragma unroll
        for (int i = 0; i < 4; ++i) {
            const float y = dpp<ROR8>(Y[i]);
            auto r = __builtin_amdgcn_permlane32_swap(__float_as_uint(y), __float_as_uint(y), false, false);
            Z[i] = Y[i] + __uint_as_float(r[1]);
        }
        if (lane < 32 && (lane & 8) == 0) ST_SC1(nosent(Z), o_pub2);
    }
    if (fin_on) {
        v4u k0, k1, k2, k3;
        float v[4];
        const float one = 1.0f;
        __builtin_amdgcn_s_sleep(10);
        POLL_PAIR(1024, v, k0, k1, k2, k3, o_fin, srd, one, one);
        const float t0 = sw32_add(v[0], v[1]), t1 = sw32_add(v[2], v[3]);
        float r = sw16_add(t0, t1);
        r += dpp<ROR8>(r);
        if ((lane & 8) == 0) s_fin[w * 32 + 8 * (2 * (rho & 1) + (rho >> 1)) + j2] = r;
        BAR();
        POLL_DRAIN(k0, k1, k2, k3);
    }
}

extern "C" void kernel_launch(void* const* d_in, const int* in_sizes, int n_in, void* d_out, int out_size, void* d_ws, size_t ws_size,
                              hipStream_t stream) {
    fused_decode<<<256, 576, 0, stream>>>((const u16*)d_in[0], (const u16*)d_in[1], (const u16*)d_in[2], (const u16*)d_in[3], (const u16*)d_in[4],
                                          (const u16*)d_in[6], (const u16*)d_in[7], (const u16*)d_in[8], (const u16*)d_in[9], (u16*)d_out,
                                          (unsigned char*)d_ws);
}
```
